```python
import torch

def _softmax(x, axis=-1):
    return torch.softmax(x, dim=axis)
def _gelu(x):
    return torch.nn.functional.gelu(x, approximate="tanh")
def _silu(x):
    return torch.nn.functional.silu(x)
def _sigmoid(x):
    return torch.sigmoid(x)
def _layernorm(x, axis=-1, eps=1e-5):
    m = x.mean(dim=axis, keepdim=True)
    v = x.var(dim=axis, keepdim=True, unbiased=False)
    return (x - m) / torch.sqrt(v + eps)
def _scatter_add(tgt, idx, src, axis=0):
    return tgt.clone().index_add_(axis, idx, src)
def _segment_sum(src, seg, n):
    out = torch.zeros((n,) + src.shape[1:], dtype=src.dtype)
    return out.index_add_(0, seg, src)
def _pad(x, pw):
    flat = tuple(v for pair in reversed(pw) for v in pair)
    return torch.nn.functional.pad(x, flat)

kernel_name = 'sequential_preLN__ssm_conv_h3__moe_arch_mod__prompt'

def setup_inputs():

    _w = (lambda a: (lambda t: t.to(torch.bfloat16) if t.is_floating_point() else t)(torch.as_tensor(a)))
    def _A_setup():
        B, S, D = 1, 2048, 1024
        ks = 4
        return {
            "q": _w((lambda *s: torch.randn(*s, dtype=torch.bfloat16) * 0.05)(B, S, D)),
            "k": _w((lambda *s: torch.randn(*s, dtype=torch.bfloat16) * 0.4)(B, S, D)),
            "v": _w((lambda *s: torch.randn(*s, dtype=torch.bfloat16) * 0.4)(B, S, D)),
            "shift_k": _w((lambda *s: torch.randn(*s, dtype=torch.bfloat16) * 0.4)(D, ks) * 0.5),
            "ssm_k": _w((lambda *s: torch.randn(*s, dtype=torch.bfloat16) * 0.4)(D, S) * (1.0 / S ** 0.5)),
            "ssm_D": _w((lambda *s: torch.randn(*s, dtype=torch.bfloat16) * 0.4)(D)),
        }
    _A_in = _A_setup()

    def _B_setup():
        B, S, D, F, C = 1, 2048, 1024, 3584, 1
        return {
            "x": _w((lambda *s: torch.randn(*s, dtype=torch.bfloat16) * 0.05)(B, S, D)),
            "w_r": _w((lambda *s: torch.randn(*s, dtype=torch.bfloat16) * 0.05)(D)),
            "w_gate": _w((lambda *s: torch.randn(*s, dtype=torch.bfloat16) * 0.05)(D, F)),
            "w_up": _w((lambda *s: torch.randn(*s, dtype=torch.bfloat16) * 0.05)(D, F)),
            "w_down": _w((lambda *s: torch.randn(*s, dtype=torch.bfloat16) * 0.05)(F, D)),
        }
    _B_in = _B_setup()

    return {
        "A_q": _A_in["q"],
        "A_k": _A_in["k"],
        "A_v": _A_in["v"],
        "A_shift_k": _A_in["shift_k"],
        "A_ssm_k": _A_in["ssm_k"],
        "A_ssm_D": _A_in["ssm_D"],
        "B_x": _B_in["x"],
        "B_w_r": _B_in["w_r"],
        "B_w_gate": _B_in["w_gate"],
        "B_w_up": _B_in["w_up"],
        "B_w_down": _B_in["w_down"],
    }

def reference(A_q, A_k, A_v, A_shift_k, A_ssm_k, A_ssm_D, B_x, B_w_r, B_w_gate, B_w_up, B_w_down):
    def _A_ref(q, k, v, shift_k, ssm_k, ssm_D):
        B, S, D = q.shape
        ks = shift_k.shape[-1]
        k_pad = _pad(k, ((0, 0), (ks - 1, 0), (0, 0)))
        k_shift = torch.zeros_like(k)
        for t in range(ks):
            k_shift = k_shift + k_pad[:, t:t + S, :] * shift_k[:, ks - 1 - t][None, None, :]
        kv = k_shift * v
        L = 2 * S
        kv_pad = _pad(kv, ((0, 0), (0, S), (0, 0)))
        kf_pad = _pad(ssm_k, ((0, 0), (0, S)))
        kv_f = (lambda x, axes=(-2,-1): torch.fft.rfft2(x.float(), dim=axes))(kv_pad[:, None, :, :], axes=(1, 2))
        kf_f = (lambda x, axes=(-2,-1): torch.fft.rfft2(x.float(), dim=axes))(kf_pad[:, None, :], axes=(1, 2))
        kf_f = (lambda x, *d: x.permute(*d))(kf_f, 1, 2, 0)
        y_f = kv_f * kf_f[None, :, :, :]
        y = (lambda x, s, axes=(-2,-1): torch.fft.irfft2(x, s=s, dim=axes).to(torch.bfloat16))(y_f, (1, L), axes=(1, 2))[:, 0, :S, :]
        y = y + kv * ssm_D[None, None, :]
        return q * y

    def _B_ref(x, w_r, w_gate, w_up, w_down):
        B, S, D = x.shape
        C = 1
        r = x @ w_r
        idx = torch.argsort(-r, axis=-1)[:, :C]
        idx_e = (lambda x, n, axis: torch.repeat_interleave(x, n, dim=axis))(idx.reshape(B, C, 1), D, axis=2)
        x_sel = (lambda x, i, axis: torch.take_along_dim(x, i, dim=axis))(x, idx_e, axis=1)
        g = _silu(x_sel @ w_gate)
        u = x_sel @ w_up
        y = (g * u) @ w_down
        r_sel = _sigmoid((lambda x, i, axis: torch.take_along_dim(x, i, dim=axis))(r, idx, axis=1))
        y = y * r_sel.reshape(B, C, 1)
        b_off = (torch.arange(B) * S).reshape(B, 1)
        flat_idx = (idx + b_off).reshape(B * C)
        zero = (x * 0.0).reshape(B * S, D)
        upd = _scatter_add(zero, flat_idx, y.reshape(B * C, D), axis=0)
        return x + upd.reshape(B, S, D)

    x = A_q
    h = x + _A_ref(_layernorm(x, axis=-1), A_k, A_v, A_shift_k, A_ssm_k, A_ssm_D)
    h2 = h + _B_ref(_layernorm(h, axis=-1), B_w_r, B_w_gate, B_w_up, B_w_down)
    return h2
```

```cpp
#include <hip/hip_runtime.h>
#include <hip/hip_bf16.h>
#include <stdint.h>

typedef unsigned short u16;
typedef unsigned int u32;
typedef u32 u32x2 __attribute__((ext_vector_type(2)));
typedef u32 u32x4 __attribute__((ext_vector_type(4)));
typedef float f32x4 __attribute__((ext_vector_type(4)));
typedef short bf16x8 __attribute__((ext_vector_type(8)));
typedef __bf16 bf16x2_t __attribute__((ext_vector_type(2)));
typedef float f32x2 __attribute__((ext_vector_type(2)));

constexpr int S = 2048, D = 1024, F = 3584;

__device__ __forceinline__ float bflo(u32 x) { return __builtin_bit_cast(float, x << 16); }
__device__ __forceinline__ float bfhi(u32 x) { return __builtin_bit_cast(float, x & 0xffff0000u); }
__device__ __forceinline__ float bf2f(u16 x) { return __builtin_bit_cast(float, (u32)x << 16); }
__device__ __forceinline__ u32 pack2(float a, float b) {
  f32x2 v = {a, b};
  bf16x2_t r = __builtin_convertvector(v, bf16x2_t);
  return __builtin_bit_cast(u32, r);
}
__device__ __forceinline__ float rbf(float a) { return bflo(pack2(a, 0.f)); }
__device__ __forceinline__ u16 f2bf(float a) { return (u16)(pack2(a, 0.f) & 0xffffu); }

__device__ __forceinline__ float wave_sum(float v) {
#pragma unroll
  for (int o = 32; o > 0; o >>= 1) v += __shfl_xor(v, o);
  return v;
}

__global__ void __launch_bounds__(256) k_kv(const u16* __restrict__ k, const u16* __restrict__ v,
                                            const u16* __restrict__ shift_k, u16* __restrict__ kvT) {
  __shared__ u16 tile[64][128 + 8];
  const int rc = blockIdx.x >> 4, cg = blockIdx.x & 15;
  const int tid = threadIdx.x;
  const int c2 = tid & 31;
  const int rs = tid >> 5;
  const int ch = cg * 64 + c2 * 2;
  const int t0 = rc * 128 + rs * 16;
  float s0[4], s1[4];
#pragma unroll
  for (int i = 0; i < 4; ++i) { s0[i] = bf2f(shift_k[ch * 4 + i]); s1[i] = bf2f(shift_k[(ch + 1) * 4 + i]); }
  u32 kp[19];
#pragma unroll
  for (int i = 0; i < 19; ++i) {
    int t = t0 - 3 + i;
    kp[i] = (t >= 0) ? *(const u32*)(k + (size_t)t * D + ch) : 0u;
  }
#pragma unroll
  for (int i = 0; i < 16; ++i) {
    u32 vv = *(const u32*)(v + (size_t)(t0 + i) * D + ch);
    float a0 = rbf(bflo(kp[i]) * s0[3]), a1 = rbf(bfhi(kp[i]) * s1[3]);
#pragma unroll
    for (int j = 1; j < 4; ++j) {
      a0 = rbf(a0 + rbf(bflo(kp[i + j]) * s0[3 - j]));
      a1 = rbf(a1 + rbf(bfhi(kp[i + j]) * s1[3 - j]));
    }
    tile[c2 * 2][rs * 16 + i] = f2bf(a0 * bflo(vv));
    tile[c2 * 2 + 1][rs * 16 + i] = f2bf(a1 * bfhi(vv));
  }
  __syncthreads();
  for (int e = tid; e < 64 * 16; e += 256) {
    int c = e >> 4, p = e & 15;
    u32x4 val = *(const u32x4*)&tile[c][p * 8];
    *(u32x4*)(kvT + (size_t)(cg * 64 + c) * S + rc * 128 + p * 8) = val;
  }
}

constexpr int HC_LEN = 2064 + 8;
constexpr int HC_STRIDE = 2080;
constexpr int XP_LEN = 2288;
__global__ void __launch_bounds__(256) k_conv(const u16* __restrict__ kvT, const u16* __restrict__ ssm_k,
                                              const u16* __restrict__ ssm_D, u16* __restrict__ y2) {
  __shared__ __attribute__((aligned(16))) u16 hc[4][4 * HC_STRIDE];
  __shared__ __attribute__((aligned(16))) u16 xp[4][XP_LEN];
  __shared__ __attribute__((aligned(16))) u16 yo[S * 4];
  const int tid = threadIdx.x, lane = tid & 63, w = tid >> 6;
  const int ch = blockIdx.x * 4 + w;
  const u16* hrow = ssm_k + (size_t)ch * S;
  const u16* xrow = kvT + (size_t)ch * S;
  for (int rho = 0; rho < 4; ++rho)
    for (int i = lane; i < HC_LEN; i += 64) {
      int s = i + rho - 16;
      hc[w][rho * HC_STRIDE + i] = (s >= 0 && s < S) ? hrow[s] : (u16)0;
    }
  for (int i = lane; i < XP_LEN; i += 64) {
    int s = (i & ~7) + (7 - (i & 7)) - 240;
    xp[w][i] = (s >= 0) ? xrow[s] : (u16)0;
  }
  __syncthreads();
  const int r = lane & 15, g = lane >> 4;
  const int rho = (1 + r) & 3;
  const u16* abase = &hc[w][rho * HC_STRIDE + (25 + r - 8 * g - rho)];
  const u16* bbase = &xp[w][(28 + 2 * r + g) * 8];
  f32x4 acc[8];
#pragma unroll
  for (int n = 0; n < 8; ++n) acc[n] = f32x4{0.f, 0.f, 0.f, 0.f};
  for (int b = 0; b < 8; ++b) {
    bf16x8 A[8], B[8];
#pragma unroll
    for (int a = 0; a < 8; ++a) {
      const u16* p = abase + 32 * (8 * a + b);
      u32x2 lo = *(const u32x2*)p, hi = *(const u32x2*)(p + 4);
      u32x4 t = {lo.x, lo.y, hi.x, hi.y};
      A[a] = __builtin_bit_cast(bf16x8, t);
      B[a] = *(const bf16x8*)(bbase + 32 * (8 * a - b));
    }
#pragma unroll
    for (int n = 0; n < 8; ++n)
#pragma unroll
      for (int a = 0; a <= n; ++a)
        acc[n] = __builtin_amdgcn_mfma_f32_16x16x32_bf16(A[a], B[n - a], acc[n], 0, 0, 0);
  }
  const float Dv = bf2f(ssm_D[ch]);
#pragma unroll
  for (int n = 0; n < 8; ++n)
#pragma unroll
    for (int j = 0; j < 4; ++j) {
      int t = 256 * n + 16 * r + 4 * g + j;
      int i = t + 240;
      float kvv = bf2f(xp[w][(i & ~7) + (7 - (i & 7))]);
      float y = rbf(acc[n][j]) + rbf(kvv * Dv);
      yo[t * 4 + w] = f2bf(y);
    }
  __syncthreads();
  for (int e = tid; e < S * 4 / 8; e += 256)
    *(u32x4*)(y2 + (size_t)blockIdx.x * S * 4 + e * 8) = *(const u32x4*)&yo[e * 8];
}

__global__ void __launch_bounds__(512) k_rows(const u16* __restrict__ q, const u16* __restrict__ y2,
                                              const u16* __restrict__ w_r, u16* __restrict__ out,
                                              float* __restrict__ cand_r, int* __restrict__ cand_i,
                                              u16* __restrict__ cand_x, u16* __restrict__ cand_h) {
  __shared__ __attribute__((aligned(16))) u16 ys[8][D];
  __shared__ float rs[8];
  const int tid = threadIdx.x, lane = tid & 63, w = tid >> 6;
  const int t0 = blockIdx.x * 8;
  for (int e = tid; e < 1024; e += 512) {
    int grp = e >> 2, p = e & 3;
    u32x4 val = *(const u32x4*)(y2 + (size_t)grp * S * 4 + (t0 + 2 * p) * 4);
    *(u32x2*)&ys[2 * p][grp * 4] = u32x2{val.x, val.y};
    *(u32x2*)&ys[2 * p + 1][grp * 4] = u32x2{val.z, val.w};
  }
  const int t = t0 + w;
  float x[16], h[16], l2[16];
  u32x4 qa[2], wa[2];
#pragma unroll
  for (int i = 0; i < 2; ++i) {
    qa[i] = *(const u32x4*)(q + (size_t)t * D + i * 512 + lane * 8);
    wa[i] = *(const u32x4*)(w_r + i * 512 + lane * 8);
  }
#pragma unroll
  for (int i = 0; i < 2; ++i)
#pragma unroll
    for (int j = 0; j < 4; ++j) { x[i * 8 + 2 * j] = bflo(qa[i][j]); x[i * 8 + 2 * j + 1] = bfhi(qa[i][j]); }
  float s = 0.f;
#pragma unroll
  for (int i = 0; i < 16; ++i) s += x[i];
  float mean = wave_sum(s) * (1.f / D);
  float s2 = 0.f;
#pragma unroll
  for (int i = 0; i < 16; ++i) { float d = x[i] - mean; s2 += d * d; }
  float var = wave_sum(s2) * (1.f / D);
  float mb = rbf(mean), sd = rbf(sqrtf(rbf(rbf(var) + 1e-5f)));
  __syncthreads();
#pragma unroll
  for (int i = 0; i < 2; ++i) {
    u32x4 yv = *(const u32x4*)&ys[w][i * 512 + lane * 8];
#pragma unroll
    for (int j = 0; j < 4; ++j) {
      float l0 = rbf(rbf(x[i * 8 + 2 * j] - mb) / sd), l1 = rbf(rbf(x[i * 8 + 2 * j + 1] - mb) / sd);
      h[i * 8 + 2 * j] = rbf(x[i * 8 + 2 * j] + rbf(l0 * bflo(yv[j])));
      h[i * 8 + 2 * j + 1] = rbf(x[i * 8 + 2 * j + 1] + rbf(l1 * bfhi(yv[j])));
    }
  }
  s = 0.f;
#pragma unroll
  for (int i = 0; i < 16; ++i) s += h[i];
  mean = wave_sum(s) * (1.f / D);
  s2 = 0.f;
#pragma unroll
  for (int i = 0; i < 16; ++i) { float d = h[i] - mean; s2 += d * d; }
  var = wave_sum(s2) * (1.f / D);
  mb = rbf(mean); sd = rbf(sqrtf(rbf(rbf(var) + 1e-5f)));
  float rr = 0.f;
#pragma unroll
  for (int i = 0; i < 2; ++i)
#pragma unroll
    for (int j = 0; j < 4; ++j) {
      l2[i * 8 + 2 * j] = rbf(rbf(h[i * 8 + 2 * j] - mb) / sd);
      l2[i * 8 + 2 * j + 1] = rbf(rbf(h[i * 8 + 2 * j + 1] - mb) / sd);
      rr += l2[i * 8 + 2 * j] * bflo(wa[i][j]) + l2[i * 8 + 2 * j + 1] * bfhi(wa[i][j]);
    }
  rr = rbf(wave_sum(rr));
#pragma unroll
  for (int i = 0; i < 2; ++i) {
    u32x4 o;
#pragma unroll
    for (int j = 0; j < 4; ++j) o[j] = pack2(h[i * 8 + 2 * j] + l2[i * 8 + 2 * j], h[i * 8 + 2 * j + 1] + l2[i * 8 + 2 * j + 1]);
    *(u32x4*)(out + (size_t)t * D + i * 512 + lane * 8) = o;
  }
  if (lane == 0) rs[w] = rr;
  __syncthreads();
  int best = 0; float bv = rs[0];
#pragma unroll
  for (int i = 1; i < 8; ++i) if (rs[i] > bv) { bv = rs[i]; best = i; }
  if (w == best) {
#pragma unroll
    for (int i = 0; i < 2; ++i) {
      u32x4 a, b;
#pragma unroll
      for (int j = 0; j < 4; ++j) {
        a[j] = pack2(l2[i * 8 + 2 * j], l2[i * 8 + 2 * j + 1]);
        b[j] = pack2(h[i * 8 + 2 * j], h[i * 8 + 2 * j + 1]);
      }
      *(u32x4*)(cand_x + (size_t)blockIdx.x * D + i * 512 + lane * 8) = a;
      *(u32x4*)(cand_h + (size_t)blockIdx.x * D + i * 512 + lane * 8) = b;
    }
    if (lane == 0) { cand_r[blockIdx.x] = bv; cand_i[blockIdx.x] = t; }
  }
}

__device__ __forceinline__ int pick_best(const float* cand_r, int lane, float& bv) {
  float v = cand_r[lane]; int b = lane;
#pragma unroll
  for (int i = 1; i < 4; ++i) { float c = cand_r[lane + 64 * i]; if (c > v) { v = c; b = lane + 64 * i; } }
#pragma unroll
  for (int o = 32; o > 0; o >>= 1) {
    float ov = __shfl_xor(v, o); int ob = __shfl_xor(b, o);
    if (ov > v || (ov == v && ob < b)) { v = ov; b = ob; }
  }
  bv = v;
  return b;
}

__global__ void __launch_bounds__(512) k_ffn(const float* __restrict__ cand_r, const u16* __restrict__ cand_x,
                                             const u16* __restrict__ wg, const u16* __restrict__ wu,
                                             const u16* __restrict__ wd, float* __restrict__ part) {
  __shared__ float red[8][32];
  __shared__ float act[16];
  const int tid = threadIdx.x, lane = tid & 63, w = tid >> 6;
  const int b = blockIdx.x;
  const int fb = (b & 7) * 28 + (b >> 3);
  const int f0 = fb * 16;
  float bv;
  const int best = pick_best(cand_r, lane, bv);
  const u16* xs = cand_x + (size_t)best * D;
  const int half = tid & 1, rowi = tid >> 1;
  float g[8], u[8];
#pragma unroll
  for (int j = 0; j < 8; ++j) { g[j] = 0.f; u[j] = 0.f; }
#pragma unroll
  for (int p = 0; p < 4; ++p) {
    int d = p * 256 + rowi;
    float xv = bf2f(xs[d]);
    u32x4 a = *(const u32x4*)(wg + (size_t)d * F + f0 + half * 8);
    u32x4 c = *(const u32x4*)(wu + (size_t)d * F + f0 + half * 8);
#pragma unroll
    for (int j = 0; j < 4; ++j) {
      g[2 * j] += xv * bflo(a[j]); g[2 * j + 1] += xv * bfhi(a[j]);
      u[2 * j] += xv * bflo(c[j]); u[2 * j + 1] += xv * bfhi(c[j]);
    }
  }
#pragma unroll
  for (int j = 0; j < 8; ++j)
#pragma unroll
    for (int o = 32; o > 1; o >>= 1) { g[j] += __shfl_xor(g[j], o); u[j] += __shfl_xor(u[j], o); }
  if (lane < 2) {
#pragma unroll
    for (int j = 0; j < 8; ++j) { red[w][half * 8 + j] = g[j]; red[w][16 + half * 8 + j] = u[j]; }
  }
  __syncthreads();
  if (tid < 16) {
    float gs = 0.f, us = 0.f;
#pragma unroll
    for (int i = 0; i < 8; ++i) { gs += red[i][tid]; us += red[i][16 + tid]; }
    gs = rbf(gs); us = rbf(us);
    float sg = rbf(gs / (1.f + __expf(-gs)));
    act[tid] = rbf(sg * us);
  }
  __syncthreads();
  float o0 = 0.f, o1 = 0.f;
#pragma unroll
  for (int j = 0; j < 16; ++j) {
    u32 wv = *(const u32*)(wd + (size_t)(f0 + j) * D + tid * 2);
    o0 += act[j] * bflo(wv); o1 += act[j] * bfhi(wv);
  }
  *(f32x2*)(part + (size_t)fb * D + tid * 2) = f32x2{o0, o1};
}

__global__ void __launch_bounds__(256) k_final(const float* __restrict__ cand_r, const int* __restrict__ cand_i,
                                               const u16* __restrict__ cand_x, const u16* __restrict__ cand_h,
                                               const float* __restrict__ part, u16* __restrict__ out) {
  __shared__ float red[4][64];
  const int tid = threadIdx.x, lane = tid & 63, w = tid >> 6;
  float bv;
  const int best = pick_best(cand_r, lane, bv);
  const int c = blockIdx.x * 64 + lane;
  float s = 0.f;
  for (int p = w; p < 224; p += 4) s += part[(size_t)p * D + c];
  red[w][lane] = s;
  __syncthreads();
  if (w == 0) {
    float y = rbf(red[0][lane] + red[1][lane] + red[2][lane] + red[3][lane]);
    float sg = rbf(1.f / (1.f + __expf(-bv)));
    y = rbf(y * sg);
    float bo = rbf(bf2f(cand_x[(size_t)best * D + c]) + y);
    out[(size_t)cand_i[best] * D + c] = f2bf(bf2f(cand_h[(size_t)best * D + c]) + bo);
  }
}

extern "C" void kernel_launch(void* const* d_in, const int* in_sizes, int n_in, void* d_out, int out_size,
                              void* d_ws, size_t ws_size, hipStream_t stream) {
  const u16* q = (const u16*)d_in[0];
  const u16* k = (const u16*)d_in[1];
  const u16* v = (const u16*)d_in[2];
  const u16* shift_k = (const u16*)d_in[3];
  const u16* ssm_k = (const u16*)d_in[4];
  const u16* ssm_D = (const u16*)d_in[5];
  const u16* w_r = (const u16*)d_in[7];
  const u16* wg = (const u16*)d_in[8];
  const u16* wu = (const u16*)d_in[9];
  const u16* wd = (const u16*)d_in[10];
  u16* out = (u16*)d_out;
  char* ws = (char*)d_ws;
  u16* kvT = (u16*)ws;
  u16* y2 = (u16*)(ws + (4 << 20));
  u16* cand_x = (u16*)(ws + (8 << 20));
  u16* cand_h = (u16*)(ws + (9 << 20));
  float* part = (float*)(ws + (10 << 20));
  float* cand_r = (float*)(ws + (11 << 20));
  int* cand_i = (int*)(ws + (11 << 20) + 4096);
  k_kv<<<256, 256, 0, stream>>>(k, v, shift_k, kvT);
  k_conv<<<256, 256, 0, stream>>>(kvT, ssm_k, ssm_D, y2);
  k_rows<<<256, 512, 0, stream>>>(q, y2, w_r, out, cand_r, cand_i, cand_x, cand_h);
  k_ffn<<<224, 512, 0, stream>>>(cand_r, cand_x, wg, wu, wd, part);
  k_final<<<16, 256, 0, stream>>>(cand_r, cand_i, cand_x, cand_h, part, out);
}
```

```cpp
#define TK 1020
#define TKO 25
#include <hip/hip_runtime.h>
#include <hip/hip_bf16.h>
#include <stdint.h>

typedef unsigned short u16;
typedef unsigned int u32;
typedef unsigned long long u64;
typedef u32 u32x2 __attribute__((ext_vector_type(2)));
typedef u32 u32x4 __attribute__((ext_vector_type(4)));
typedef u64 u64x4 __attribute__((ext_vector_type(4)));
typedef float f32x2 __attribute__((ext_vector_type(2)));
typedef float f32x4 __attribute__((ext_vector_type(4)));
typedef short s16x4 __attribute__((ext_vector_type(4)));
typedef short bf16x8 __attribute__((ext_vector_type(8)));
typedef __bf16 bf16x2_t __attribute__((ext_vector_type(2)));
typedef __amdgpu_buffer_rsrc_t rsrc_t;
#define GAS __attribute__((address_space(1)))
#define LAS __attribute__((address_space(3)))
#define CAS __attribute__((address_space(4)))

constexpr int S = 2048, D = 1024, F = 3584;
constexpr int NB = 256, NT = 512;
constexpr int NFX = 14;
#define NREP 8
#define KST 16
constexpr u32 KPG = KST * 128 < 4096 ? 4096 : KST * 128;
constexpr u32 SENT = 0xFFFFFFFFu;
constexpr int SPIN_CAP = 1 << 16;
constexpr int SC1 = 16;

constexpr u32 WS_E = 0, WS_FLAG = 4096, WS_TB = 8192, WS_F2 = 16384, WS_KEY = 65536, WS_CAND = 1u << 20, WS_X = 2u << 20, WS_CK = 8u << 20, WS_END = 9u << 20;
#define XPAD 0
constexpr u32 XS = 16384 + XPAD;
#define STAMP(i)
#define STAMPH(i)
#define STAMPW(i, W)
#define STAMP_INIT()
#define STAMP_DUMP()
#define L_EXTRA 0

#define STAMPF(i)
#define STAMPY(i)
constexpr int HC_CP = 4160, HC_CH = 4 * HC_CP, XP_CH = 4576, YS_CH = 4128, RW_ST = 2080;
constexpr int L_HC = 0, L_XP = 4 * HC_CH, L_YS = L_XP + 4 * XP_CH, L_RW = L_YS + 4 * YS_CH, L_XS = L_RW + 8 * RW_ST,
              L_KEY = L_XS + 2048, L_SYN = L_KEY + 64, L_RED = L_SYN + 448, L_Q = L_RED + 1024, L_END = L_Q + 16384;

__device__ __forceinline__ float bflo(u32 x) { return __builtin_bit_cast(float, x << 16); }
__device__ __forceinline__ float bfhi(u32 x) { return __builtin_bit_cast(float, x & 0xffff0000u); }
__device__ __forceinline__ u32 pack2(float a, float b) {
  f32x2 v = {a, b};
  return __builtin_bit_cast(u32, __builtin_convertvector(v, bf16x2_t));
}
__device__ __forceinline__ float reven(float a) { return __builtin_bit_cast(float, (__builtin_bit_cast(u32, a) + 0x10000u) & 0xfffe0000u); }
__device__ __forceinline__ float rbf(float a) { return __builtin_bit_cast(float, pack2(0.f, a)); }
__device__ __forceinline__ float dot2(u32 a, u32 b, float c) {
  return __builtin_amdgcn_fdot2_f32_bf16(__builtin_bit_cast(bf16x2_t, a), __builtin_bit_cast(bf16x2_t, b), c, false);
}
template <int CTRL> __device__ __forceinline__ float dppf(float x) {
  return __builtin_bit_cast(float, __builtin_amdgcn_mov_dpp(__builtin_bit_cast(int, x), CTRL, 0xf, 0xf, true));
}
template <int CTRL> __device__ __forceinline__ u32 dppu(u32 x) { return (u32)__builtin_amdgcn_mov_dpp((int)x, CTRL, 0xf, 0xf, true); }
constexpr int XOR1 = 0xB1, XOR2 = 0x4E, XOR7 = 0x141, XOR8 = 0x128;
__device__ __forceinline__ float xrow_sum(float x) {
  auto s = __builtin_amdgcn_permlane16_swap(__builtin_bit_cast(u32, x), __builtin_bit_cast(u32, x), false, false);
  x = __builtin_bit_cast(float, (u32)s[0]) + __builtin_bit_cast(float, (u32)s[1]);
  auto t = __builtin_amdgcn_permlane32_swap(__builtin_bit_cast(u32, x), __builtin_bit_cast(u32, x), false, false);
  return __builtin_bit_cast(float, (u32)t[0]) + __builtin_bit_cast(float, (u32)t[1]);
}
__device__ __forceinline__ float wave_sum(float x) {
  x += dppf<XOR1>(x); x += dppf<XOR2>(x); x += dppf<XOR7>(x); x += dppf<XOR8>(x);
  return xrow_sum(x);
}
__device__ __forceinline__ float half_sum(float a, float b) {
  auto t = __builtin_amdgcn_permlane32_swap(__builtin_bit_cast(u32, a), __builtin_bit_cast(u32, b), false, false);
  float x = __builtin_bit_cast(float, (u32)t[0]) + __builtin_bit_cast(float, (u32)t[1]);
  auto s = __builtin_amdgcn_permlane16_swap(__builtin_bit_cast(u32, x), __builtin_bit_cast(u32, x), false, false);
  x = __builtin_bit_cast(float, (u32)s[0]) + __builtin_bit_cast(float, (u32)s[1]);
  x += dppf<XOR1>(x); x += dppf<XOR2>(x); x += dppf<XOR7>(x); x += dppf<XOR8>(x);
  return x;
}
__device__ __forceinline__ float rdl(float x, int l) { return __builtin_bit_cast(float, __builtin_amdgcn_readlane(__builtin_bit_cast(int, x), l)); }
__device__ __forceinline__ f32x2 unpk(u32 x) { return f32x2{bflo(x), bfhi(x)}; }
__device__ __forceinline__ u32 pk(f32x2 v) { return __builtin_bit_cast(u32, __builtin_convertvector(v, bf16x2_t)); }
__device__ __forceinline__ u32 umax(u32 a, u32 b) { return a > b ? a : b; }
__device__ __forceinline__ u32 umin(u32 a, u32 b) { return a < b ? a : b; }
__device__ __forceinline__ u32 wave_umax(u32 x) {
  x = umax(x, dppu<XOR1>(x)); x = umax(x, dppu<XOR2>(x)); x = umax(x, dppu<XOR7>(x)); x = umax(x, dppu<XOR8>(x));
  auto s = __builtin_amdgcn_permlane16_swap(x, x, false, false);
  x = umax(s[0], s[1]);
  auto t = __builtin_amdgcn_permlane32_swap(x, x, false, false);
  return umax(t[0], t[1]);
}
__device__ __forceinline__ rsrc_t mk(const void* p, u32 bytes) { return __builtin_amdgcn_make_buffer_rsrc((void*)p, 0, bytes, 0x00020000); }
template <int AUX> __device__ __forceinline__ u32x4 ld128(rsrc_t r, u32 off, u32 soff = 0) { return __builtin_amdgcn_raw_buffer_load_b128(r, off, soff, AUX); }
template <int AUX> __device__ __forceinline__ u32x2 ld64(rsrc_t r, u32 off, u32 soff = 0) { return __builtin_amdgcn_raw_buffer_load_b64(r, off, soff, AUX); }
template <int AUX> __device__ __forceinline__ u32 ld32(rsrc_t r, u32 off, u32 soff = 0) { return __builtin_amdgcn_raw_buffer_load_b32(r, off, soff, AUX); }
template <int AUX> __device__ __forceinline__ void st128(u32x4 v, rsrc_t r, u32 off, u32 soff = 0) { __builtin_amdgcn_raw_buffer_store_b128(v, r, off + soff, 0, AUX); }
template <int AUX> __device__ __forceinline__ void st64(u32x2 v, rsrc_t r, u32 off, u32 soff = 0) { __builtin_amdgcn_raw_buffer_store_b64(v, r, off, soff, AUX); }
template <int AUX> __device__ __forceinline__ void st32(u32 v, rsrc_t r, u32 off, u32 soff = 0) { __builtin_amdgcn_raw_buffer_store_b32(v, r, off, soff, AUX); }
__device__ __forceinline__ u32x4 guard(u32x4 v) {
  return u32x4{umin(v.x, SENT - 1), umin(v.y, SENT - 1), umin(v.z, SENT - 1), umin(v.w, SENT - 1)};
}
__device__ __forceinline__ u32 rotr(u32 x, int r) { return __builtin_amdgcn_alignbit(x, x, r); }
__device__ __forceinline__ u32x2 check(u32x4 a, u32x4 c, u64 mix) {
  return u32x2{((u32)mix + a.x + a.y) + (a.z + a.w + c.x) + (c.y + c.z + c.w),
               ((u32)(mix >> 32) + rotr(a.x, 1) + rotr(a.y, 3)) + (rotr(a.z, 5) + rotr(a.w, 7) + rotr(c.x, 9)) +
                   (rotr(c.y, 11) + rotr(c.z, 13) + rotr(c.w, 15))};
}
__device__ __forceinline__ u32 vmax4(u32x4 v) { return umax(umax(v.x, v.y), umax(v.z, v.w)); }
__device__ __forceinline__ f32x4 mfma(u32x4 a, u32x4 b, f32x4 c) {
  return __builtin_amdgcn_mfma_f32_16x16x32_bf16(__builtin_bit_cast(bf16x8, a), __builtin_bit_cast(bf16x8, b), c, 0, 0, 0);
}
typedef u32 u32x8 __attribute__((ext_vector_type(8)));
typedef __bf16 bf16x8_t __attribute__((ext_vector_type(8)));
typedef __bf16 bf16x16_t __attribute__((ext_vector_type(16)));
__device__ __forceinline__ f32x4 smfma(u32x4 a, u32x8 b, f32x4 c, u32 idx) {
  return __builtin_amdgcn_smfmac_f32_16x16x64_bf16(__builtin_bit_cast(bf16x8_t, a), __builtin_bit_cast(bf16x16_t, b), c, idx, 0, 0);
}
#define BAR() asm volatile("s_waitcnt lgkmcnt(0)\n\ts_barrier" ::: "memory")
#define SB() __builtin_amdgcn_sched_barrier(0)
#define KEEP(x) asm volatile("" : "+v"(x))
#define LDS128(off) (*(LAS u32x4*)(lds + (off)))
#define LDS64(off) (*(LAS u32x2*)(lds + (off)))
#define LDS32(off) (*(LAS u32*)(lds + (off)))

__device__ __forceinline__ float rstd(float var) {
  return __builtin_amdgcn_rcpf(rbf(__builtin_amdgcn_sqrtf(rbf(rbf(var) + 1e-5f))));
}

#define TLBP 0
#define TOFF 0
#define T3Q 8
#define QH0 0
#define T3M 10
#define HP2 0
#define FPR 3
#define SPOS 1
#define EFL 1
#define HS2 99
#define T2E 8
#define TQE 0
#define CKC 0
#define TKA 885
#define ZE 4
#define TS0 0
#define TS1 0
#define TS2 0
#define VS2 0
#define GPK 0
#define GPC 0x89aedcu
#define TGP 850
#define GPD 0
#define WORD 1
#define TOD 0
#define TO0 1000
#define TO1 30
#define TOP 2
#define KTD 0
#define RMS 16
#define TREP 2
#define CKK 0
#define CKKC 30
#define CKR 420
#define TP3 0
#define P3K 4
#define SHF 0
#define TLN 0
#define XPO 0
#define FW 1
#define FAUX 0
#define YWS 0
#define NSET 3
#define PDL 4
#define PCLOB "v100", "v101", "v102", "v103", "v104", "v105", "v106", "v107", "v108", "v109", "v110", "v111", "v112", "v113", "v114", "v115", "v116", "v117", "v118", "v119", "v120", "v121", "v122", "v123", "v124", "v125", "v126", "v127", "v128", "v129", "v130", "v131", "v132", "v133", "v134", "v135", "v136", "v137", "v138", "v139", "v140", "v141", "v142", "v143", "v144", "v145", "v146", "v147"
#define POLLH 0
#define PH4 0
#define TWD 4
#define TL1 470
#define TL2 520
#define LDO 1
#define TK2 60
#define TRE 1
#define HSL2 1
#define TD2 0
#define TF2 0
#define HPR 3
#define TRW 3
#define FNO 0
#define FOR 0
#define WDQ 0
#define BE 2048
#define NST 0x2888
#define RST 0x3068
#define WP3 2
#define QH 0
#define TD1 850
#define QAL 1
#define WE0 0
#define TP 870
#define WF 1
#define G4E 1
#define FLW (((NST >> 12) ? 4 : (NST >> 8) ? 3 : 2) - 2 - G4E)
#define QLT 1
#define KQ 1
#define TC 742
#define DT 46
#define PH 6
#define PHN 12
#define LAG 9
#define CBS 0
#define PUBJ 31
#define CB2 1
#define CW 3
#define CA 0
#define HSL 0
#define T3A 20
#define TR 140
#define CL 20
#define PD 0
#define XNO 0
#define PSY 32
#define QSY 64
#define KW 0
#define PW 1
#define LW 1
#define QEND 4
#define CSEQ 20
#define NCK 7
#define CONV_ASM \
    "ds_read_b128 v[128:131], %[bb] offset:448\n\t" \
    "ds_read_b64 v[132:133], %[ab] offset:0\n\t" \
    "ds_read_b64 v[134:135], %[ab] offset:8\n\t" \
    "ds_read_b128 v[136:139], %[bb] offset:960\n\t" \
    "ds_read_b64 v[140:141], %[ab] offset:512\n\t" \
    "ds_read_b64 v[142:143], %[ab] offset:520\n\t" \
    "ds_read_b128 v[144:147], %[bb] offset:1472\n\t" \
    "ds_read_b64 v[148:149], %[ab] offset:1024\n\t" \
    "ds_read_b64 v[150:151], %[ab] offset:1032\n\t" \
    "ds_read_b128 v[152:155], %[bb] offset:1984\n\t" \
    "ds_read_b64 v[156:157], %[ab] offset:1536\n\t" \
    "ds_read_b64 v[158:159], %[ab] offset:1544\n\t" \
    "s_waitcnt lgkmcnt(9)\n\t" \
    "v_mfma_f32_16x16x32_bf16 v[96:99], v[132:135], v[128:131], 0\n\t" \
    "ds_read_b128 v[160:163], %[bb] offset:2496\n\t" \
    "s_waitcnt lgkmcnt(9)\n\t" \
    "v_mfma_f32_16x16x32_bf16 v[100:103], v[132:135], v[136:139], 0\n\t" \
    "ds_read_b64 v[164:165], %[ab] offset:2048\n\t" \
    "s_waitcnt lgkmcnt(8)\n\t" \
    "v_mfma_f32_16x16x32_bf16 v[100:103], v[140:143], v[128:131], v[100:103]\n\t" \
    "ds_read_b64 v[166:167], %[ab] offset:2056\n\t" \
    "s_waitcnt lgkmcnt(8)\n\t" \
    "v_mfma_f32_16x16x32_bf16 v[104:107], v[132:135], v[144:147], 0\n\t" \
    "ds_read_b128 v[168:171], %[bb] offset:3008\n\t" \
    "v_mfma_f32_16x16x32_bf16 v[104:107], v[140:143], v[136:139], v[104:107]\n\t" \
    "ds_read_b64 v[172:173], %[ab] offset:2560\n\t" \
    "s_waitcnt lgkmcnt(8)\n\t" \
    "v_mfma_f32_16x16x32_bf16 v[104:107], v[148:151], v[128:131], v[104:107]\n\t" \
    "ds_read_b64 v[174:175], %[ab] offset:2568\n\t" \
    "s_waitcnt lgkmcnt(8)\n\t" \
    "v_mfma_f32_16x16x32_bf16 v[108:111], v[132:135], v[152:155], 0\n\t" \
    "ds_read_b128 v[176:179], %[bb] offset:3520\n\t" \
    "v_mfma_f32_16x16x32_bf16 v[108:111], v[140:143], v[144:147], v[108:111]\n\t" \
    "ds_read_b64 v[180:181], %[ab] offset:3072\n\t" \
    "v_mfma_f32_16x16x32_bf16 v[108:111], v[148:151], v[136:139], v[108:111]\n\t" \
    "ds_read_b64 v[182:183], %[ab] offset:3080\n\t" \
    "s_waitcnt lgkmcnt(9)\n\t" \
    "v_mfma_f32_16x16x32_bf16 v[108:111], v[156:159], v[128:131], v[108:111]\n\t" \
    "ds_read_b128 v[184:187], %[bb] offset:4032\n\t" \
    "s_waitcnt lgkmcnt(9)\n\t" \
    "v_mfma_f32_16x16x32_bf16 v[112:115], v[132:135], v[160:163], 0\n\t" \
    "ds_read_b64 v[188:189], %[ab] offset:3584\n\t" \
    "v_mfma_f32_16x16x32_bf16 v[112:115], v[140:143], v[152:155], v[112:115]\n\t" \
    "ds_read_b64 v[190:191], %[ab] offset:3592\n\t" \
    "v_mfma_f32_16x16x32_bf16 v[112:115], v[148:151], v[144:147], v[112:115]\n\t" \
    "ds_read_b128 v[192:195], %[bb] offset:384\n\t" \
    "v_mfma_f32_16x16x32_bf16 v[112:115], v[156:159], v[136:139], v[112:115]\n\t" \
    "ds_read_b64 v[196:197], %[ab] offset:64\n\t" \
    "s_waitcnt lgkmcnt(9)\n\t" \
    "v_mfma_f32_16x16x32_bf16 v[112:115], v[164:167], v[128:131], v[112:115]\n\t" \
    "ds_read_b64 v[198:199], %[ab] offset:72\n\t" \
    "v_mfma_f32_16x16x32_bf16 v[116:119], v[132:135], v[168:171], 0\n\t" \
    "ds_read_b128 v[200:203], %[bb] offset:896\n\t" \
    "v_mfma_f32_16x16x32_bf16 v[116:119], v[140:143], v[160:163], v[116:119]\n\t" \
    "ds_read_b64 v[204:205], %[ab] offset:576\n\t" \
    "v_mfma_f32_16x16x32_bf16 v[116:119], v[148:151], v[152:155], v[116:119]\n\t" \
    "ds_read_b64 v[206:207], %[ab] offset:584\n\t" \
    "v_mfma_f32_16x16x32_bf16 v[116:119], v[156:159], v[144:147], v[116:119]\n\t" \
    "ds_read_b128 v[208:211], %[bb] offset:1408\n\t" \
    "v_mfma_f32_16x16x32_bf16 v[116:119], v[164:167], v[136:139], v[116:119]\n\t" \
    "ds_read_b64 v[212:213], %[ab] offset:1088\n\t" \
    "s_waitcnt lgkmcnt(9)\n\t" \
    "v_mfma_f32_16x16x32_bf16 v[116:119], v[172:175], v[128:131], v[116:119]\n\t" \
    "ds_read_b64 v[214:215], %[ab] offset:1096\n\t" \
    "v_mfma_f32_16x16x32_bf16 v[120:123], v[132:135], v[176:179], 0\n\t" \
    "ds_read_b128 v[216:219], %[bb] offset:1920\n\t" \
    "v_mfma_f32_16x16x32_bf16 v[120:123], v[140:143], v[168:171], v[120:123]\n\t" \
    "ds_read_b64 v[220:221], %[ab] offset:1600\n\t" \
    "v_mfma_f32_16x16x32_bf16 v[120:123], v[148:151], v[160:163], v[120:123]\n\t" \
    "ds_read_b64 v[222:223], %[ab] offset:1608\n\t" \
    "v_mfma_f32_16x16x32_bf16 v[120:123], v[156:159], v[152:155], v[120:123]\n\t" \
    "ds_read_b128 v[224:227], %[bb] offset:2432\n\t" \
    "v_mfma_f32_16x16x32_bf16 v[120:123], v[164:167], v[144:147], v[120:123]\n\t" \
    "ds_read_b64 v[228:229], %[ab] offset:2112\n\t" \
    "v_mfma_f32_16x16x32_bf16 v[120:123], v[172:175], v[136:139], v[120:123]\n\t" \
    "ds_read_b64 v[230:231], %[ab] offset:2120\n\t" \
    "v_mfma_f32_16x16x32_bf16 v[120:123], v[180:183], v[128:131], v[120:123]\n\t" \
    "s_memrealtime s[80:81]\n\t" \
    "ds_read_b128 v[232:235], %[bb] offset:2944\n\t" \
    "v_mfma_f32_16x16x32_bf16 v[124:127], v[132:135], v[184:187], 0\n\t" \
    "ds_read_b64 v[236:237], %[ab] offset:2624\n\t" \
    "v_mfma_f32_16x16x32_bf16 v[124:127], v[140:143], v[176:179], v[124:127]\n\t" \
    "ds_read_b64 v[238:239], %[ab] offset:2632\n\t" \
    "v_mfma_f32_16x16x32_bf16 v[124:127], v[148:151], v[168:171], v[124:127]\n\t" \
    "ds_read_b128 v[240:243], %[bb] offset:3456\n\t" \
    "v_mfma_f32_16x16x32_bf16 v[124:127], v[156:159], v[160:163], v[124:127]\n\t" \
    "ds_read_b64 v[244:245], %[ab] offset:3136\n\t" \
    "v_mfma_f32_16x16x32_bf16 v[124:127], v[164:167], v[152:155], v[124:127]\n\t" \
    "ds_read_b64 v[246:247], %[ab] offset:3144\n\t" \
    "v_mfma_f32_16x16x32_bf16 v[124:127], v[172:175], v[144:147], v[124:127]\n\t" \
    "ds_read_b128 v[248:251], %[bb] offset:3968\n\t" \
    "v_mfma_f32_16x16x32_bf16 v[124:127], v[180:183], v[136:139], v[124:127]\n\t" \
    "ds_read_b64 v[252:253], %[ab] offset:3648\n\t" \
    "s_waitcnt lgkmcnt(9)\n\t" \
    "v_mfma_f32_16x16x32_bf16 v[124:127], v[188:191], v[128:131], v[124:127]\n\t" \
    "ds_read_b64 v[254:255], %[ab] offset:3656\n\t" \
    "v_mfma_f32_16x16x32_bf16 v[96:99], v[196:199], v[192:195], v[96:99]\n\t" \
    "s_cmp_lt_u32 s80, %[tg]\n\t" \
    "s_cbranch_scc1 conv_w0h_%=\n\t" \
    "conv_b0h_%=:\n\t" \
    "v_mfma_f32_16x16x32_bf16 v[100:103], v[196:199], v[200:203], v[100:103]\n\t" \
    "s_add_u32 %[tg], %[tg], %[dt]\n\t" \
    "ds_read_b128 v[132:135], %[bb] offset:320\n\t" \
    "v_mfma_f32_16x16x32_bf16 v[100:103], v[204:207], v[192:195], v[100:103]\n\t" \
    "ds_read_b64 v[184:185], %[ab] offset:128\n\t" \
    "v_mfma_f32_16x16x32_bf16 v[104:107], v[196:199], v[208:211], v[104:107]\n\t" \
    "ds_read_b64 v[186:187], %[ab] offset:136\n\t" \
    "v_mfma_f32_16x16x32_bf16 v[104:107], v[204:207], v[200:203], v[104:107]\n\t" \
    "ds_read_b128 v[140:143], %[bb] offset:832\n\t" \
    "v_mfma_f32_16x16x32_bf16 v[104:107], v[212:215], v[192:195], v[104:107]\n\t" \
    "ds_read_b64 v[176:177], %[ab] offset:640\n\t" \
    "v_mfma_f32_16x16x32_bf16 v[108:111], v[196:199], v[216:219], v[108:111]\n\t" \
    "ds_read_b64 v[178:179], %[ab] offset:648\n\t" \
    "v_mfma_f32_16x16x32_bf16 v[108:111], v[204:207], v[208:211], v[108:111]\n\t" \
    "ds_read_b128 v[148:151], %[bb] offset:1344\n\t" \
    "v_mfma_f32_16x16x32_bf16 v[108:111], v[212:215], v[200:203], v[108:111]\n\t" \
    "ds_read_b64 v[168:169], %[ab] offset:1152\n\t" \
    "v_mfma_f32_16x16x32_bf16 v[108:111], v[220:223], v[192:195], v[108:111]\n\t" \
    "ds_read_b64 v[170:171], %[ab] offset:1160\n\t" \
    "v_mfma_f32_16x16x32_bf16 v[112:115], v[196:199], v[224:227], v[112:115]\n\t" \
    "ds_read_b128 v[156:159], %[bb] offset:1856\n\t" \
    "v_mfma_f32_16x16x32_bf16 v[112:115], v[204:207], v[216:219], v[112:115]\n\t" \
    "ds_read_b64 v[160:161], %[ab] offset:1664\n\t" \
    "v_mfma_f32_16x16x32_bf16 v[112:115], v[212:215], v[208:211], v[112:115]\n\t" \
    "ds_read_b64 v[162:163], %[ab] offset:1672\n\t" \
    "v_mfma_f32_16x16x32_bf16 v[112:115], v[220:223], v[200:203], v[112:115]\n\t" \
    "ds_read_b128 v[152:155], %[bb] offset:2368\n\t" \
    "s_waitcnt lgkmcnt(9)\n\t" \
    "v_mfma_f32_16x16x32_bf16 v[112:115], v[228:231], v[192:195], v[112:115]\n\t" \
    "ds_read_b64 v[164:165], %[ab] offset:2176\n\t" \
    "v_mfma_f32_16x16x32_bf16 v[116:119], v[196:199], v[232:235], v[116:119]\n\t" \
    "ds_read_b64 v[166:167], %[ab] offset:2184\n\t" \
    "v_mfma_f32_16x16x32_bf16 v[116:119], v[204:207], v[224:227], v[116:119]\n\t" \
    "ds_read_b128 v[144:147], %[bb] offset:2880\n\t" \
    "v_mfma_f32_16x16x32_bf16 v[116:119], v[212:215], v[216:219], v[116:119]\n\t" \
    "ds_read_b64 v[172:173], %[ab] offset:2688\n\t" \
    "v_mfma_f32_16x16x32_bf16 v[116:119], v[220:223], v[208:211], v[116:119]\n\t" \
    "ds_read_b64 v[174:175], %[ab] offset:2696\n\t" \
    "v_mfma_f32_16x16x32_bf16 v[116:119], v[228:231], v[200:203], v[116:119]\n\t" \
    "ds_read_b128 v[136:139], %[bb] offset:3392\n\t" \
    "v_mfma_f32_16x16x32_bf16 v[116:119], v[236:239], v[192:195], v[116:119]\n\t" \
    "ds_read_b64 v[180:181], %[ab] offset:3200\n\t" \
    "v_mfma_f32_16x16x32_bf16 v[120:123], v[196:199], v[240:243], v[120:123]\n\t" \
    "ds_read_b64 v[182:183], %[ab] offset:3208\n\t" \
    "v_mfma_f32_16x16x32_bf16 v[120:123], v[204:207], v[232:235], v[120:123]\n\t" \
    "ds_read_b128 v[128:131], %[bb] offset:3904\n\t" \
    "v_mfma_f32_16x16x32_bf16 v[120:123], v[212:215], v[224:227], v[120:123]\n\t" \
    "ds_read_b64 v[188:189], %[ab] offset:3712\n\t" \
    "v_mfma_f32_16x16x32_bf16 v[120:123], v[220:223], v[216:219], v[120:123]\n\t" \
    "ds_read_b64 v[190:191], %[ab] offset:3720\n\t" \
    "v_mfma_f32_16x16x32_bf16 v[120:123], v[228:231], v[208:211], v[120:123]\n\t" \
    "v_mfma_f32_16x16x32_bf16 v[120:123], v[236:239], v[200:203], v[120:123]\n\t" \
    "v_mfma_f32_16x16x32_bf16 v[120:123], v[244:247], v[192:195], v[120:123]\n\t" \
    "s_memrealtime s[80:81]\n\t" \
    "v_mfma_f32_16x16x32_bf16 v[124:127], v[196:199], v[248:251], v[124:127]\n\t" \
    "v_mfma_f32_16x16x32_bf16 v[124:127], v[204:207], v[240:243], v[124:127]\n\t" \
    "ds_read_b128 v[196:199], %[bb] offset:256\n\t" \
    "v_mfma_f32_16x16x32_bf16 v[124:127], v[212:215], v[232:235], v[124:127]\n\t" \
    "ds_read_b64 v[248:249], %[ab] offset:192\n\t" \
    "v_mfma_f32_16x16x32_bf16 v[124:127], v[220:223], v[224:227], v[124:127]\n\t" \
    "ds_read_b64 v[250:251], %[ab] offset:200\n\t" \
    "v_mfma_f32_16x16x32_bf16 v[124:127], v[228:231], v[216:219], v[124:127]\n\t" \
    "ds_read_b128 v[204:207], %[bb] offset:768\n\t" \
    "v_mfma_f32_16x16x32_bf16 v[124:127], v[236:239], v[208:211], v[124:127]\n\t" \
    "ds_read_b64 v[240:241], %[ab] offset:704\n\t" \
    "v_mfma_f32_16x16x32_bf16 v[124:127], v[244:247], v[200:203], v[124:127]\n\t" \
    "ds_read_b64 v[242:243], %[ab] offset:712\n\t" \
    "v_mfma_f32_16x16x32_bf16 v[124:127], v[252:255], v[192:195], v[124:127]\n\t" \
    "ds_read_b128 v[212:215], %[bb] offset:1280\n\t" \
    "v_mfma_f32_16x16x32_bf16 v[96:99], v[184:187], v[132:135], v[96:99]\n\t" \
    "s_cmp_lt_u32 s80, %[tg]\n\t" \
    "s_cbranch_scc1 conv_w1h_%=\n\t" \
    "conv_b1h_%=:\n\t" \
    "v_mfma_f32_16x16x32_bf16 v[100:103], v[184:187], v[140:143], v[100:103]\n\t" \
    "s_add_u32 %[tg], %[tg], %[dt]\n\t" \
    "s_waitcnt lgkmcnt(7)\n\t" \
    "v_mfma_f32_16x16x32_bf16 v[100:103], v[176:179], v[132:135], v[100:103]\n\t" \
    "ds_read_b64 v[232:233], %[ab] offset:1216\n\t" \
    "v_mfma_f32_16x16x32_bf16 v[104:107], v[184:187], v[148:151], v[104:107]\n\t" \
    "ds_read_b64 v[234:235], %[ab] offset:1224\n\t" \
    "v_mfma_f32_16x16x32_bf16 v[104:107], v[176:179], v[140:143], v[104:107]\n\t" \
    "ds_read_b128 v[220:223], %[bb] offset:1792\n\t" \
    "v_mfma_f32_16x16x32_bf16 v[104:107], v[168:171], v[132:135], v[104:107]\n\t" \
    "ds_read_b64 v[224:225], %[ab] offset:1728\n\t" \
    "v_mfma_f32_16x16x32_bf16 v[108:111], v[184:187], v[156:159], v[108:111]\n\t" \
    "ds_read_b64 v[226:227], %[ab] offset:1736\n\t" \
    "v_mfma_f32_16x16x32_bf16 v[108:111], v[176:179], v[148:151], v[108:111]\n\t" \
    "ds_read_b128 v[216:219], %[bb] offset:2304\n\t" \
    "v_mfma_f32_16x16x32_bf16 v[108:111], v[168:171], v[140:143], v[108:111]\n\t" \
    "ds_read_b64 v[228:229], %[ab] offset:2240\n\t" \
    "v_mfma_f32_16x16x32_bf16 v[108:111], v[160:163], v[132:135], v[108:111]\n\t" \
    "ds_read_b64 v[230:231], %[ab] offset:2248\n\t" \
    "v_mfma_f32_16x16x32_bf16 v[112:115], v[184:187], v[152:155], v[112:115]\n\t" \
    "ds_read_b128 v[208:211], %[bb] offset:2816\n\t" \
    "v_mfma_f32_16x16x32_bf16 v[112:115], v[176:179], v[156:159], v[112:115]\n\t" \
    "ds_read_b64 v[236:237], %[ab] offset:2752\n\t" \
    "v_mfma_f32_16x16x32_bf16 v[112:115], v[168:171], v[148:151], v[112:115]\n\t" \
    "ds_read_b64 v[238:239], %[ab] offset:2760\n\t" \
    "v_mfma_f32_16x16x32_bf16 v[112:115], v[160:163], v[140:143], v[112:115]\n\t" \
    "ds_read_b128 v[200:203], %[bb] offset:3328\n\t" \
    "v_mfma_f32_16x16x32_bf16 v[112:115], v[164:167], v[132:135], v[112:115]\n\t" \
    "ds_read_b64 v[244:245], %[ab] offset:3264\n\t" \
    "v_mfma_f32_16x16x32_bf16 v[116:119], v[184:187], v[144:147], v[116:119]\n\t" \
    "ds_read_b64 v[246:247], %[ab] offset:3272\n\t" \
    "v_mfma_f32_16x16x32_bf16 v[116:119], v[176:179], v[152:155], v[116:119]\n\t" \
    "ds_read_b128 v[192:195], %[bb] offset:3840\n\t" \
    "v_mfma_f32_16x16x32_bf16 v[116:119], v[168:171], v[156:159], v[116:119]\n\t" \
    "ds_read_b64 v[252:253], %[ab] offset:3776\n\t" \
    "v_mfma_f32_16x16x32_bf16 v[116:119], v[160:163], v[148:151], v[116:119]\n\t" \
    "ds_read_b64 v[254:255], %[ab] offset:3784\n\t" \
    "v_mfma_f32_16x16x32_bf16 v[116:119], v[164:167], v[140:143], v[116:119]\n\t" \
    "v_mfma_f32_16x16x32_bf16 v[116:119], v[172:175], v[132:135], v[116:119]\n\t" \
    "v_mfma_f32_16x16x32_bf16 v[120:123], v[184:187], v[136:139], v[120:123]\n\t" \
    "v_mfma_f32_16x16x32_bf16 v[120:123], v[176:179], v[144:147], v[120:123]\n\t" \
    "v_mfma_f32_16x16x32_bf16 v[120:123], v[168:171], v[152:155], v[120:123]\n\t" \
    "v_mfma_f32_16x16x32_bf16 v[120:123], v[160:163], v[156:159], v[120:123]\n\t" \
    "v_mfma_f32_16x16x32_bf16 v[120:123], v[164:167], v[148:151], v[120:123]\n\t" \
    "v_mfma_f32_16x16x32_bf16 v[120:123], v[172:175], v[140:143], v[120:123]\n\t" \
    "v_mfma_f32_16x16x32_bf16 v[120:123], v[180:183], v[132:135], v[120:123]\n\t" \
    "s_memrealtime s[80:81]\n\t" \
    "v_mfma_f32_16x16x32_bf16 v[124:127], v[184:187], v[128:131], v[124:127]\n\t" \
    "v_mfma_f32_16x16x32_bf16 v[124:127], v[176:179], v[136:139], v[124:127]\n\t" \
    "ds_read_b128 v[128:131], %[bb] offset:192\n\t" \
    "v_mfma_f32_16x16x32_bf16 v[124:127], v[168:171], v[144:147], v[124:127]\n\t" \
    "ds_read_b64 v[184:185], %[ab] offset:256\n\t" \
    "v_mfma_f32_16x16x32_bf16 v[124:127], v[160:163], v[152:155], v[124:127]\n\t" \
    "ds_read_b64 v[186:187], %[ab] offset:264\n\t" \
    "v_mfma_f32_16x16x32_bf16 v[124:127], v[164:167], v[156:159], v[124:127]\n\t" \
    "ds_read_b128 v[136:139], %[bb] offset:704\n\t" \
    "v_mfma_f32_16x16x32_bf16 v[124:127], v[172:175], v[148:151], v[124:127]\n\t" \
    "ds_read_b64 v[176:177], %[ab] offset:768\n\t" \
    "v_mfma_f32_16x16x32_bf16 v[124:127], v[180:183], v[140:143], v[124:127]\n\t" \
    "ds_read_b64 v[178:179], %[ab] offset:776\n\t" \
    "v_mfma_f32_16x16x32_bf16 v[124:127], v[188:191], v[132:135], v[124:127]\n\t" \
    "ds_read_b128 v[144:147], %[bb] offset:1216\n\t" \
    "s_waitcnt lgkmcnt(7)\n\t" \
    "v_mfma_f32_16x16x32_bf16 v[96:99], v[248:251], v[196:199], v[96:99]\n\t" \
    "s_cmp_lt_u32 s80, %[tg]\n\t" \
    "s_cbranch_scc1 conv_w2h_%=\n\t" \
    "conv_b2h_%=:\n\t" \
    "v_mfma_f32_16x16x32_bf16 v[100:103], v[248:251], v[204:207], v[100:103]\n\t" \
    "s_add_u32 %[tg], %[tg], %[dt]\n\t" \
    "ds_read_b64 v[168:169], %[ab] offset:1280\n\t" \
    "v_mfma_f32_16x16x32_bf16 v[100:103], v[240:243], v[196:199], v[100:103]\n\t" \
    "ds_read_b64 v[170:171], %[ab] offset:1288\n\t" \
    "v_mfma_f32_16x16x32_bf16 v[104:107], v[248:251], v[212:215], v[104:107]\n\t" \
    "ds_read_b128 v[152:155], %[bb] offset:1728\n\t" \
    "v_mfma_f32_16x16x32_bf16 v[104:107], v[240:243], v[204:207], v[104:107]\n\t" \
    "ds_read_b64 v[160:161], %[ab] offset:1792\n\t" \
    "v_mfma_f32_16x16x32_bf16 v[104:107], v[232:235], v[196:199], v[104:107]\n\t" \
    "ds_read_b64 v[162:163], %[ab] offset:1800\n\t" \
    "v_mfma_f32_16x16x32_bf16 v[108:111], v[248:251], v[220:223], v[108:111]\n\t" \
    "ds_read_b128 v[156:159], %[bb] offset:2240\n\t" \
    "v_mfma_f32_16x16x32_bf16 v[108:111], v[240:243], v[212:215], v[108:111]\n\t" \
    "ds_read_b64 v[164:165], %[ab] offset:2304\n\t" \
    "v_mfma_f32_16x16x32_bf16 v[108:111], v[232:235], v[204:207], v[108:111]\n\t" \
    "ds_read_b64 v[166:167], %[ab] offset:2312\n\t" \
    "v_mfma_f32_16x16x32_bf16 v[108:111], v[224:227], v[196:199], v[108:111]\n\t" \
    "ds_read_b128 v[148:151], %[bb] offset:2752\n\t" \
    "v_mfma_f32_16x16x32_bf16 v[112:115], v[248:251], v[216:219], v[112:115]\n\t" \
    "ds_read_b64 v[172:173], %[ab] offset:2816\n\t" \
    "v_mfma_f32_16x16x32_bf16 v[112:115], v[240:243], v[220:223], v[112:115]\n\t" \
    "ds_read_b64 v[174:175], %[ab] offset:2824\n\t" \
    "v_mfma_f32_16x16x32_bf16 v[112:115], v[232:235], v[212:215], v[112:115]\n\t" \
    "ds_read_b128 v[140:143], %[bb] offset:3264\n\t" \
    "v_mfma_f32_16x16x32_bf16 v[112:115], v[224:227], v[204:207], v[112:115]\n\t" \
    "ds_read_b64 v[180:181], %[ab] offset:3328\n\t" \
    "v_mfma_f32_16x16x32_bf16 v[112:115], v[228:231], v[196:199], v[112:115]\n\t" \
    "ds_read_b64 v[182:183], %[ab] offset:3336\n\t" \
    "v_mfma_f32_16x16x32_bf16 v[116:119], v[248:251], v[208:211], v[116:119]\n\t" \
    "ds_read_b128 v[132:135], %[bb] offset:3776\n\t" \
    "v_mfma_f32_16x16x32_bf16 v[116:119], v[240:243], v[216:219], v[116:119]\n\t" \
    "ds_read_b64 v[188:189], %[ab] offset:3840\n\t" \
    "v_mfma_f32_16x16x32_bf16 v[116:119], v[232:235], v[220:223], v[116:119]\n\t" \
    "ds_read_b64 v[190:191], %[ab] offset:3848\n\t" \
    "v_mfma_f32_16x16x32_bf16 v[116:119], v[224:227], v[212:215], v[116:119]\n\t" \
    "v_mfma_f32_16x16x32_bf16 v[116:119], v[228:231], v[204:207], v[116:119]\n\t" \
    "v_mfma_f32_16x16x32_bf16 v[116:119], v[236:239], v[196:199], v[116:119]\n\t" \
    "v_mfma_f32_16x16x32_bf16 v[120:123], v[248:251], v[200:203], v[120:123]\n\t" \
    "v_mfma_f32_16x16x32_bf16 v[120:123], v[240:243], v[208:211], v[120:123]\n\t" \
    "v_mfma_f32_16x16x32_bf16 v[120:123], v[232:235], v[216:219], v[120:123]\n\t" \
    "v_mfma_f32_16x16x32_bf16 v[120:123], v[224:227], v[220:223], v[120:123]\n\t" \
    "v_mfma_f32_16x16x32_bf16 v[120:123], v[228:231], v[212:215], v[120:123]\n\t" \
    "v_mfma_f32_16x16x32_bf16 v[120:123], v[236:239], v[204:207], v[120:123]\n\t" \
    "v_mfma_f32_16x16x32_bf16 v[120:123], v[244:247], v[196:199], v[120:123]\n\t" \
    "s_memrealtime s[80:81]\n\t" \
    "v_mfma_f32_16x16x32_bf16 v[124:127], v[248:251], v[192:195], v[124:127]\n\t" \
    "v_mfma_f32_16x16x32_bf16 v[124:127], v[240:243], v[200:203], v[124:127]\n\t" \
    "ds_read_b128 v[192:195], %[bb] offset:128\n\t" \
    "v_mfma_f32_16x16x32_bf16 v[124:127], v[232:235], v[208:211], v[124:127]\n\t" \
    "ds_read_b64 v[248:249], %[ab] offset:320\n\t" \
    "v_mfma_f32_16x16x32_bf16 v[124:127], v[224:227], v[216:219], v[124:127]\n\t" \
    "ds_read_b64 v[250:251], %[ab] offset:328\n\t" \
    "v_mfma_f32_16x16x32_bf16 v[124:127], v[228:231], v[220:223], v[124:127]\n\t" \
    "ds_read_b128 v[200:203], %[bb] offset:640\n\t" \
    "v_mfma_f32_16x16x32_bf16 v[124:127], v[236:239], v[212:215], v[124:127]\n\t" \
    "ds_read_b64 v[240:241], %[ab] offset:832\n\t" \
    "v_mfma_f32_16x16x32_bf16 v[124:127], v[244:247], v[204:207], v[124:127]\n\t" \
    "ds_read_b64 v[242:243], %[ab] offset:840\n\t" \
    "v_mfma_f32_16x16x32_bf16 v[124:127], v[252:255], v[196:199], v[124:127]\n\t" \
    "ds_read_b128 v[208:211], %[bb] offset:1152\n\t" \
    "s_waitcnt lgkmcnt(7)\n\t" \
    "v_mfma_f32_16x16x32_bf16 v[96:99], v[184:187], v[128:131], v[96:99]\n\t" \
    "s_cmp_lt_u32 s80, %[tg]\n\t" \
    "s_cbranch_scc1 conv_w3h_%=\n\t" \
    "conv_b3h_%=:\n\t" \
    "v_mfma_f32_16x16x32_bf16 v[100:103], v[184:187], v[136:139], v[100:103]\n\t" \
    "s_add_u32 %[tg], %[tg], %[dt]\n\t" \
    "ds_read_b64 v[232:233], %[ab] offset:1344\n\t" \
    "v_mfma_f32_16x16x32_bf16 v[100:103], v[176:179], v[128:131], v[100:103]\n\t" \
    "ds_read_b64 v[234:235], %[ab] offset:1352\n\t" \
    "v_mfma_f32_16x16x32_bf16 v[104:107], v[184:187], v[144:147], v[104:107]\n\t" \
    "ds_read_b128 v[216:219], %[bb] offset:1664\n\t" \
    "v_mfma_f32_16x16x32_bf16 v[104:107], v[176:179], v[136:139], v[104:107]\n\t" \
    "ds_read_b64 v[224:225], %[ab] offset:1856\n\t" \
    "v_mfma_f32_16x16x32_bf16 v[104:107], v[168:171], v[128:131], v[104:107]\n\t" \
    "ds_read_b64 v[226:227], %[ab] offset:1864\n\t" \
    "v_mfma_f32_16x16x32_bf16 v[108:111], v[184:187], v[152:155], v[108:111]\n\t" \
    "ds_read_b128 v[220:223], %[bb] offset:2176\n\t" \
    "v_mfma_f32_16x16x32_bf16 v[108:111], v[176:179], v[144:147], v[108:111]\n\t" \
    "ds_read_b64 v[228:229], %[ab] offset:2368\n\t" \
    "v_mfma_f32_16x16x32_bf16 v[108:111], v[168:171], v[136:139], v[108:111]\n\t" \
    "ds_read_b64 v[230:231], %[ab] offset:2376\n\t" \
    "v_mfma_f32_16x16x32_bf16 v[108:111], v[160:163], v[128:131], v[108:111]\n\t" \
    "ds_read_b128 v[212:215], %[bb] offset:2688\n\t" \
    "v_mfma_f32_16x16x32_bf16 v[112:115], v[184:187], v[156:159], v[112:115]\n\t" \
    "ds_read_b64 v[236:237], %[ab] offset:2880\n\t" \
    "v_mfma_f32_16x16x32_bf16 v[112:115], v[176:179], v[152:155], v[112:115]\n\t" \
    "ds_read_b64 v[238:239], %[ab] offset:2888\n\t" \
    "v_mfma_f32_16x16x32_bf16 v[112:115], v[168:171], v[144:147], v[112:115]\n\t" \
    "ds_read_b128 v[204:207], %[bb] offset:3200\n\t" \
    "v_mfma_f32_16x16x32_bf16 v[112:115], v[160:163], v[136:139], v[112:115]\n\t" \
    "ds_read_b64 v[244:245], %[ab] offset:3392\n\t" \
    "v_mfma_f32_16x16x32_bf16 v[112:115], v[164:167], v[128:131], v[112:115]\n\t" \
    "ds_read_b64 v[246:247], %[ab] offset:3400\n\t" \
    "v_mfma_f32_16x16x32_bf16 v[116:119], v[184:187], v[148:151], v[116:119]\n\t" \
    "ds_read_b128 v[196:199], %[bb] offset:3712\n\t" \
    "v_mfma_f32_16x16x32_bf16 v[116:119], v[176:179], v[156:159], v[116:119]\n\t" \
    "ds_read_b64 v[252:253], %[ab] offset:3904\n\t" \
    "v_mfma_f32_16x16x32_bf16 v[116:119], v[168:171], v[152:155], v[116:119]\n\t" \
    "ds_read_b64 v[254:255], %[ab] offset:3912\n\t" \
    "v_mfma_f32_16x16x32_bf16 v[116:119], v[160:163], v[144:147], v[116:119]\n\t" \
    "v_mfma_f32_16x16x32_bf16 v[116:119], v[164:167], v[136:139], v[116:119]\n\t" \
    "v_mfma_f32_16x16x32_bf16 v[116:119], v[172:175], v[128:131], v[116:119]\n\t" \
    "v_mfma_f32_16x16x32_bf16 v[120:123], v[184:187], v[140:143], v[120:123]\n\t" \
    "v_mfma_f32_16x16x32_bf16 v[120:123], v[176:179], v[148:151], v[120:123]\n\t" \
    "v_mfma_f32_16x16x32_bf16 v[120:123], v[168:171], v[156:159], v[120:123]\n\t" \
    "v_mfma_f32_16x16x32_bf16 v[120:123], v[160:163], v[152:155], v[120:123]\n\t" \
    "v_mfma_f32_16x16x32_bf16 v[120:123], v[164:167], v[144:147], v[120:123]\n\t" \
    "v_mfma_f32_16x16x32_bf16 v[120:123], v[172:175], v[136:139], v[120:123]\n\t" \
    "v_mfma_f32_16x16x32_bf16 v[120:123], v[180:183], v[128:131], v[120:123]\n\t" \
    "s_memrealtime s[80:81]\n\t" \
    "v_mfma_f32_16x16x32_bf16 v[124:127], v[184:187], v[132:135], v[124:127]\n\t" \
    "v_mfma_f32_16x16x32_bf16 v[124:127], v[176:179], v[140:143], v[124:127]\n\t" \
    "ds_read_b128 v[132:135], %[bb] offset:64\n\t" \
    "v_mfma_f32_16x16x32_bf16 v[124:127], v[168:171], v[148:151], v[124:127]\n\t" \
    "ds_read_b64 v[184:185], %[ab] offset:384\n\t" \
    "v_mfma_f32_16x16x32_bf16 v[124:127], v[160:163], v[156:159], v[124:127]\n\t" \
    "ds_read_b64 v[186:187], %[ab] offset:392\n\t" \
    "v_mfma_f32_16x16x32_bf16 v[124:127], v[164:167], v[152:155], v[124:127]\n\t" \
    "ds_read_b128 v[140:143], %[bb] offset:576\n\t" \
    "v_mfma_f32_16x16x32_bf16 v[124:127], v[172:175], v[144:147], v[124:127]\n\t" \
    "ds_read_b64 v[176:177], %[ab] offset:896\n\t" \
    "v_mfma_f32_16x16x32_bf16 v[124:127], v[180:183], v[136:139], v[124:127]\n\t" \
    "ds_read_b64 v[178:179], %[ab] offset:904\n\t" \
    "v_mfma_f32_16x16x32_bf16 v[124:127], v[188:191], v[128:131], v[124:127]\n\t" \
    "ds_read_b128 v[148:151], %[bb] offset:1088\n\t" \
    "s_waitcnt lgkmcnt(7)\n\t" \
    "v_mfma_f32_16x16x32_bf16 v[96:99], v[248:251], v[192:195], v[96:99]\n\t" \
    "s_cmp_lt_u32 s80, %[tg]\n\t" \
    "s_cbranch_scc1 conv_w4h_%=\n\t" \
    "conv_b4h_%=:\n\t" \
    "v_mfma_f32_16x16x32_bf16 v[100:103], v[248:251], v[200:203], v[100:103]\n\t" \
    "s_add_u32 %[tg], %[tg], %[dt]\n\t" \
    "ds_read_b64 v[168:169], %[ab] offset:1408\n\t" \
    "v_mfma_f32_16x16x32_bf16 v[100:103], v[240:243], v[192:195], v[100:103]\n\t" \
    "ds_read_b64 v[170:171], %[ab] offset:1416\n\t" \
    "v_mfma_f32_16x16x32_bf16 v[104:107], v[248:251], v[208:211], v[104:107]\n\t" \
    "ds_read_b128 v[156:159], %[bb] offset:1600\n\t" \
    "v_mfma_f32_16x16x32_bf16 v[104:107], v[240:243], v[200:203], v[104:107]\n\t" \
    "ds_read_b64 v[160:161], %[ab] offset:1920\n\t" \
    "v_mfma_f32_16x16x32_bf16 v[104:107], v[232:235], v[192:195], v[104:107]\n\t" \
    "ds_read_b64 v[162:163], %[ab] offset:1928\n\t" \
    "v_mfma_f32_16x16x32_bf16 v[108:111], v[248:251], v[216:219], v[108:111]\n\t" \
    "ds_read_b128 v[152:155], %[bb] offset:2112\n\t" \
    "v_mfma_f32_16x16x32_bf16 v[108:111], v[240:243], v[208:211], v[108:111]\n\t" \
    "ds_read_b64 v[164:165], %[ab] offset:2432\n\t" \
    "v_mfma_f32_16x16x32_bf16 v[108:111], v[232:235], v[200:203], v[108:111]\n\t" \
    "ds_read_b64 v[166:167], %[ab] offset:2440\n\t" \
    "v_mfma_f32_16x16x32_bf16 v[108:111], v[224:227], v[192:195], v[108:111]\n\t" \
    "ds_read_b128 v[144:147], %[bb] offset:2624\n\t" \
    "v_mfma_f32_16x16x32_bf16 v[112:115], v[248:251], v[220:223], v[112:115]\n\t" \
    "ds_read_b64 v[172:173], %[ab] offset:2944\n\t" \
    "v_mfma_f32_16x16x32_bf16 v[112:115], v[240:243], v[216:219], v[112:115]\n\t" \
    "ds_read_b64 v[174:175], %[ab] offset:2952\n\t" \
    "v_mfma_f32_16x16x32_bf16 v[112:115], v[232:235], v[208:211], v[112:115]\n\t" \
    "ds_read_b128 v[136:139], %[bb] offset:3136\n\t" \
    "v_mfma_f32_16x16x32_bf16 v[112:115], v[224:227], v[200:203], v[112:115]\n\t" \
    "ds_read_b64 v[180:181], %[ab] offset:3456\n\t" \
    "v_mfma_f32_16x16x32_bf16 v[112:115], v[228:231], v[192:195], v[112:115]\n\t" \
    "ds_read_b64 v[182:183], %[ab] offset:3464\n\t" \
    "v_mfma_f32_16x16x32_bf16 v[116:119], v[248:251], v[212:215], v[116:119]\n\t" \
    "ds_read_b128 v[128:131], %[bb] offset:3648\n\t" \
    "v_mfma_f32_16x16x32_bf16 v[116:119], v[240:243], v[220:223], v[116:119]\n\t" \
    "ds_read_b64 v[188:189], %[ab] offset:3968\n\t" \
    "v_mfma_f32_16x16x32_bf16 v[116:119], v[232:235], v[216:219], v[116:119]\n\t" \
    "ds_read_b64 v[190:191], %[ab] offset:3976\n\t" \
    "v_mfma_f32_16x16x32_bf16 v[116:119], v[224:227], v[208:211], v[116:119]\n\t" \
    "v_mfma_f32_16x16x32_bf16 v[116:119], v[228:231], v[200:203], v[116:119]\n\t" \
    "v_mfma_f32_16x16x32_bf16 v[116:119], v[236:239], v[192:195], v[116:119]\n\t" \
    "v_mfma_f32_16x16x32_bf16 v[120:123], v[248:251], v[204:207], v[120:123]\n\t" \
    "v_mfma_f32_16x16x32_bf16 v[120:123], v[240:243], v[212:215], v[120:123]\n\t" \
    "v_mfma_f32_16x16x32_bf16 v[120:123], v[232:235], v[220:223], v[120:123]\n\t" \
    "v_mfma_f32_16x16x32_bf16 v[120:123], v[224:227], v[216:219], v[120:123]\n\t" \
    "v_mfma_f32_16x16x32_bf16 v[120:123], v[228:231], v[208:211], v[120:123]\n\t" \
    "v_mfma_f32_16x16x32_bf16 v[120:123], v[236:239], v[200:203], v[120:123]\n\t" \
    "v_mfma_f32_16x16x32_bf16 v[120:123], v[244:247], v[192:195], v[120:123]\n\t" \
    "s_memrealtime s[80:81]\n\t" \
    "v_mfma_f32_16x16x32_bf16 v[124:127], v[248:251], v[196:199], v[124:127]\n\t" \
    "v_mfma_f32_16x16x32_bf16 v[124:127], v[240:243], v[204:207], v[124:127]\n\t" \
    "ds_read_b128 v[196:199], %[bb] offset:0\n\t" \
    "v_mfma_f32_16x16x32_bf16 v[124:127], v[232:235], v[212:215], v[124:127]\n\t" \
    "ds_read_b64 v[248:249], %[ab] offset:448\n\t" \
    "v_mfma_f32_16x16x32_bf16 v[124:127], v[224:227], v[220:223], v[124:127]\n\t" \
    "ds_read_b64 v[250:251], %[ab] offset:456\n\t" \
    "v_mfma_f32_16x16x32_bf16 v[124:127], v[228:231], v[216:219], v[124:127]\n\t" \
    "ds_read_b128 v[204:207], %[bb] offset:512\n\t" \
    "v_mfma_f32_16x16x32_bf16 v[124:127], v[236:239], v[208:211], v[124:127]\n\t" \
    "ds_read_b64 v[240:241], %[ab] offset:960\n\t" \
    "v_mfma_f32_16x16x32_bf16 v[124:127], v[244:247], v[200:203], v[124:127]\n\t" \
    "ds_read_b64 v[242:243], %[ab] offset:968\n\t" \
    "v_mfma_f32_16x16x32_bf16 v[124:127], v[252:255], v[192:195], v[124:127]\n\t" \
    "ds_read_b128 v[212:215], %[bb] offset:1024\n\t" \
    "s_waitcnt lgkmcnt(7)\n\t" \
    "v_mfma_f32_16x16x32_bf16 v[96:99], v[184:187], v[132:135], v[96:99]\n\t" \
    "s_cmp_lt_u32 s80, %[tg]\n\t" \
    "s_cbranch_scc1 conv_w5h_%=\n\t" \
    "conv_b5h_%=:\n\t" \
    "v_mfma_f32_16x16x32_bf16 v[100:103], v[184:187], v[140:143], v[100:103]\n\t" \
    "s_add_u32 %[tg], %[tg], %[dt]\n\t" \
    "ds_read_b64 v[232:233], %[ab] offset:1472\n\t" \
    "v_mfma_f32_16x16x32_bf16 v[100:103], v[176:179], v[132:135], v[100:103]\n\t" \
    "ds_read_b64 v[234:235], %[ab] offset:1480\n\t" \
    "v_mfma_f32_16x16x32_bf16 v[104:107], v[184:187], v[148:151], v[104:107]\n\t" \
    "ds_read_b128 v[220:223], %[bb] offset:1536\n\t" \
    "v_mfma_f32_16x16x32_bf16 v[104:107], v[176:179], v[140:143], v[104:107]\n\t" \
    "ds_read_b64 v[224:225], %[ab] offset:1984\n\t" \
    "v_mfma_f32_16x16x32_bf16 v[104:107], v[168:171], v[132:135], v[104:107]\n\t" \
    "ds_read_b64 v[226:227], %[ab] offset:1992\n\t" \
    "v_mfma_f32_16x16x32_bf16 v[108:111], v[184:187], v[156:159], v[108:111]\n\t" \
    "ds_read_b128 v[216:219], %[bb] offset:2048\n\t" \
    "v_mfma_f32_16x16x32_bf16 v[108:111], v[176:179], v[148:151], v[108:111]\n\t" \
    "ds_read_b64 v[228:229], %[ab] offset:2496\n\t" \
    "v_mfma_f32_16x16x32_bf16 v[108:111], v[168:171], v[140:143], v[108:111]\n\t" \
    "ds_read_b64 v[230:231], %[ab] offset:2504\n\t" \
    "v_mfma_f32_16x16x32_bf16 v[108:111], v[160:163], v[132:135], v[108:111]\n\t" \
    "ds_read_b128 v[208:211], %[bb] offset:2560\n\t" \
    "v_mfma_f32_16x16x32_bf16 v[112:115], v[184:187], v[152:155], v[112:115]\n\t" \
    "ds_read_b64 v[236:237], %[ab] offset:3008\n\t" \
    "v_mfma_f32_16x16x32_bf16 v[112:115], v[176:179], v[156:159], v[112:115]\n\t" \
    "ds_read_b64 v[238:239], %[ab] offset:3016\n\t" \
    "v_mfma_f32_16x16x32_bf16 v[112:115], v[168:171], v[148:151], v[112:115]\n\t" \
    "ds_read_b128 v[200:203], %[bb] offset:3072\n\t" \
    "v_mfma_f32_16x16x32_bf16 v[112:115], v[160:163], v[140:143], v[112:115]\n\t" \
    "ds_read_b64 v[244:245], %[ab] offset:3520\n\t" \
    "v_mfma_f32_16x16x32_bf16 v[112:115], v[164:167], v[132:135], v[112:115]\n\t" \
    "ds_read_b64 v[246:247], %[ab] offset:3528\n\t" \
    "v_mfma_f32_16x16x32_bf16 v[116:119], v[184:187], v[144:147], v[116:119]\n\t" \
    "ds_read_b128 v[192:195], %[bb] offset:3584\n\t" \
    "v_mfma_f32_16x16x32_bf16 v[116:119], v[176:179], v[152:155], v[116:119]\n\t" \
    "ds_read_b64 v[252:253], %[ab] offset:4032\n\t" \
    "v_mfma_f32_16x16x32_bf16 v[116:119], v[168:171], v[156:159], v[116:119]\n\t" \
    "ds_read_b64 v[254:255], %[ab] offset:4040\n\t" \
    "v_mfma_f32_16x16x32_bf16 v[116:119], v[160:163], v[148:151], v[116:119]\n\t" \
    "v_mfma_f32_16x16x32_bf16 v[116:119], v[164:167], v[140:143], v[116:119]\n\t" \
    "v_mfma_f32_16x16x32_bf16 v[116:119], v[172:175], v[132:135], v[116:119]\n\t" \
    "v_mfma_f32_16x16x32_bf16 v[120:123], v[184:187], v[136:139], v[120:123]\n\t" \
    "v_mfma_f32_16x16x32_bf16 v[120:123], v[176:179], v[144:147], v[120:123]\n\t" \
    "v_mfma_f32_16x16x32_bf16 v[120:123], v[168:171], v[152:155], v[120:123]\n\t" \
    "v_mfma_f32_16x16x32_bf16 v[120:123], v[160:163], v[156:159], v[120:123]\n\t" \
    "v_mfma_f32_16x16x32_bf16 v[120:123], v[164:167], v[148:151], v[120:123]\n\t" \
    "v_mfma_f32_16x16x32_bf16 v[120:123], v[172:175], v[140:143], v[120:123]\n\t" \
    "v_mfma_f32_16x16x32_bf16 v[120:123], v[180:183], v[132:135], v[120:123]\n\t" \
    "s_memrealtime s[80:81]\n\t" \
    "v_mfma_f32_16x16x32_bf16 v[124:127], v[184:187], v[128:131], v[124:127]\n\t" \
    "v_mfma_f32_16x16x32_bf16 v[124:127], v[176:179], v[136:139], v[124:127]\n\t" \
    "v_mfma_f32_16x16x32_bf16 v[124:127], v[168:171], v[144:147], v[124:127]\n\t" \
    "v_mfma_f32_16x16x32_bf16 v[124:127], v[160:163], v[152:155], v[124:127]\n\t" \
    "v_mfma_f32_16x16x32_bf16 v[124:127], v[164:167], v[156:159], v[124:127]\n\t" \
    "v_mfma_f32_16x16x32_bf16 v[124:127], v[172:175], v[148:151], v[124:127]\n\t" \
    "v_mfma_f32_16x16x32_bf16 v[124:127], v[180:183], v[140:143], v[124:127]\n\t" \
    "v_mfma_f32_16x16x32_bf16 v[124:127], v[188:191], v[132:135], v[124:127]\n\t" \
    "s_cmp_eq_u32 %[wv], 0\n\t" \
    "s_cbranch_scc1 conv_tr_%=\n\t" \
    "s_waitcnt lgkmcnt(0)\n\t" \
    "v_mfma_f32_16x16x32_bf16 v[96:99], v[248:251], v[196:199], v[96:99]\n\t" \
    "s_cmp_lt_u32 s80, %[tg]\n\t" \
    "s_cbranch_scc1 conv_w6c_%=\n\t" \
    "conv_b6c_%=:\n\t" \
    "v_mfma_f32_16x16x32_bf16 v[100:103], v[248:251], v[204:207], v[100:103]\n\t" \
    "v_mfma_f32_16x16x32_bf16 v[100:103], v[240:243], v[196:199], v[100:103]\n\t" \
    "v_mfma_f32_16x16x32_bf16 v[104:107], v[248:251], v[212:215], v[104:107]\n\t" \
    "v_mfma_f32_16x16x32_bf16 v[104:107], v[240:243], v[204:207], v[104:107]\n\t" \
    "v_mfma_f32_16x16x32_bf16 v[104:107], v[232:235], v[196:199], v[104:107]\n\t" \
    "v_mfma_f32_16x16x32_bf16 v[108:111], v[248:251], v[220:223], v[108:111]\n\t" \
    "v_cvt_pk_bf16_f32 v96, v96, v97\n\t" \
    "v_cvt_pk_bf16_f32 v97, v98, v99\n\t" \
    "v_mfma_f32_16x16x32_bf16 v[108:111], v[240:243], v[212:215], v[108:111]\n\t" \
    "ds_write_b64 %[yb], v[96:97] offset:0\n\t" \
    "v_cvt_pk_bf16_f32 v100, v100, v101\n\t" \
    "v_mfma_f32_16x16x32_bf16 v[108:111], v[232:235], v[204:207], v[108:111]\n\t" \
    "v_cvt_pk_bf16_f32 v101, v102, v103\n\t" \
    "ds_write_b64 %[yb], v[100:101] offset:512\n\t" \
    "v_mfma_f32_16x16x32_bf16 v[108:111], v[224:227], v[196:199], v[108:111]\n\t" \
    "v_cvt_pk_bf16_f32 v104, v104, v105\n\t" \
    "v_cvt_pk_bf16_f32 v105, v106, v107\n\t" \
    "v_mfma_f32_16x16x32_bf16 v[112:115], v[248:251], v[216:219], v[112:115]\n\t" \
    "ds_write_b64 %[yb], v[104:105] offset:1024\n\t" \
    "v_mfma_f32_16x16x32_bf16 v[112:115], v[240:243], v[220:223], v[112:115]\n\t" \
    "v_mfma_f32_16x16x32_bf16 v[112:115], v[232:235], v[212:215], v[112:115]\n\t" \
    "v_mfma_f32_16x16x32_bf16 v[112:115], v[224:227], v[204:207], v[112:115]\n\t" \
    "v_mfma_f32_16x16x32_bf16 v[112:115], v[228:231], v[196:199], v[112:115]\n\t" \
    "v_cvt_pk_bf16_f32 v108, v108, v109\n\t" \
    "v_cvt_pk_bf16_f32 v109, v110, v111\n\t" \
    "v_mfma_f32_16x16x32_bf16 v[116:119], v[248:251], v[208:211], v[116:119]\n\t" \
    "ds_write_b64 %[yb], v[108:109] offset:1536\n\t" \
    "v_mfma_f32_16x16x32_bf16 v[116:119], v[240:243], v[216:219], v[116:119]\n\t" \
    "v_mfma_f32_16x16x32_bf16 v[116:119], v[232:235], v[220:223], v[116:119]\n\t" \
    "ds_write_b32 %[fa], %[fa]\n\t" \
    "v_mfma_f32_16x16x32_bf16 v[116:119], v[224:227], v[212:215], v[116:119]\n\t" \
    "v_cvt_pk_bf16_f32 v112, v112, v113\n\t" \
    "v_cvt_pk_bf16_f32 v113, v114, v115\n\t" \
    "v_mfma_f32_16x16x32_bf16 v[116:119], v[228:231], v[204:207], v[116:119]\n\t" \
    "ds_write_b64 %[yb], v[112:113] offset:2048\n\t" \
    "v_mfma_f32_16x16x32_bf16 v[116:119], v[236:239], v[196:199], v[116:119]\n\t" \
    "v_mfma_f32_16x16x32_bf16 v[120:123], v[248:251], v[200:203], v[120:123]\n\t" \
    "v_mfma_f32_16x16x32_bf16 v[120:123], v[240:243], v[208:211], v[120:123]\n\t" \
    "v_mfma_f32_16x16x32_bf16 v[120:123], v[232:235], v[216:219], v[120:123]\n\t" \
    "v_mfma_f32_16x16x32_bf16 v[120:123], v[224:227], v[220:223], v[120:123]\n\t" \
    "v_mfma_f32_16x16x32_bf16 v[120:123], v[228:231], v[212:215], v[120:123]\n\t" \
    "v_mfma_f32_16x16x32_bf16 v[120:123], v[236:239], v[204:207], v[120:123]\n\t" \
    "v_mfma_f32_16x16x32_bf16 v[120:123], v[244:247], v[196:199], v[120:123]\n\t" \
    "v_mfma_f32_16x16x32_bf16 v[124:127], v[248:251], v[192:195], v[124:127]\n\t" \
    "v_cvt_pk_bf16_f32 v116, v116, v117\n\t" \
    "v_cvt_pk_bf16_f32 v117, v118, v119\n\t" \
    "v_mfma_f32_16x16x32_bf16 v[124:127], v[240:243], v[200:203], v[124:127]\n\t" \
    "ds_write_b64 %[yb], v[116:117] offset:2560\n\t" \
    "v_mfma_f32_16x16x32_bf16 v[124:127], v[232:235], v[208:211], v[124:127]\n\t" \
    "v_mfma_f32_16x16x32_bf16 v[124:127], v[224:227], v[216:219], v[124:127]\n\t" \
    "v_mfma_f32_16x16x32_bf16 v[124:127], v[228:231], v[220:223], v[124:127]\n\t" \
    "v_cvt_pk_bf16_f32 v120, v120, v121\n\t" \
    "v_cvt_pk_bf16_f32 v121, v122, v123\n\t" \
    "v_mfma_f32_16x16x32_bf16 v[124:127], v[236:239], v[212:215], v[124:127]\n\t" \
    "ds_write_b64 %[yb], v[120:121] offset:3072\n\t" \
    "v_mfma_f32_16x16x32_bf16 v[124:127], v[244:247], v[204:207], v[124:127]\n\t" \
    "v_mfma_f32_16x16x32_bf16 v[124:127], v[252:255], v[196:199], v[124:127]\n\t" \
    "s_nop 7\n\t" \
    "v_cvt_pk_bf16_f32 v124, v124, v125\n\t" \
    "v_cvt_pk_bf16_f32 v125, v126, v127\n\t" \
    "ds_write_b64 %[yb], v[124:125] offset:3584\n\t" \
    "s_branch conv_end_%=\n\t" \
    "conv_w0h_%=:\n\t" \
    "s_memrealtime s[80:81]\n\t" \
    "s_waitcnt lgkmcnt(0)\n\t" \
    "s_sub_u32 s80, s80, %[tg]\n\t" \
    "s_cmp_lt_i32 s80, %[lag]\n\t" \
    "s_cbranch_scc1 conv_w0h_%=\n\t" \
    "s_branch conv_b0h_%=\n\t" \
    "conv_w1h_%=:\n\t" \
    "s_memrealtime s[80:81]\n\t" \
    "s_waitcnt lgkmcnt(0)\n\t" \
    "s_sub_u32 s80, s80, %[tg]\n\t" \
    "s_cmp_lt_i32 s80, %[lag]\n\t" \
    "s_cbranch_scc1 conv_w1h_%=\n\t" \
    "s_branch conv_b1h_%=\n\t" \
    "conv_w2h_%=:\n\t" \
    "s_memrealtime s[80:81]\n\t" \
    "s_waitcnt lgkmcnt(0)\n\t" \
    "s_sub_u32 s80, s80, %[tg]\n\t" \
    "s_cmp_lt_i32 s80, %[lag]\n\t" \
    "s_cbranch_scc1 conv_w2h_%=\n\t" \
    "s_branch conv_b2h_%=\n\t" \
    "conv_w3h_%=:\n\t" \
    "s_memrealtime s[80:81]\n\t" \
    "s_waitcnt lgkmcnt(0)\n\t" \
    "s_sub_u32 s80, s80, %[tg]\n\t" \
    "s_cmp_lt_i32 s80, %[lag]\n\t" \
    "s_cbranch_scc1 conv_w3h_%=\n\t" \
    "s_branch conv_b3h_%=\n\t" \
    "conv_w4h_%=:\n\t" \
    "s_memrealtime s[80:81]\n\t" \
    "s_waitcnt lgkmcnt(0)\n\t" \
    "s_sub_u32 s80, s80, %[tg]\n\t" \
    "s_cmp_lt_i32 s80, %[lag]\n\t" \
    "s_cbranch_scc1 conv_w4h_%=\n\t" \
    "s_branch conv_b4h_%=\n\t" \
    "conv_w5h_%=:\n\t" \
    "s_memrealtime s[80:81]\n\t" \
    "s_waitcnt lgkmcnt(0)\n\t" \
    "s_sub_u32 s80, s80, %[tg]\n\t" \
    "s_cmp_lt_i32 s80, %[lag]\n\t" \
    "s_cbranch_scc1 conv_w5h_%=\n\t" \
    "s_branch conv_b5h_%=\n\t" \
    "conv_w6c_%=:\n\t" \
    "s_memrealtime s[80:81]\n\t" \
    "s_waitcnt lgkmcnt(0)\n\t" \
    "s_sub_u32 s80, s80, %[tg]\n\t" \
    "s_cmp_lt_i32 s80, %[lag]\n\t" \
    "s_cbranch_scc1 conv_w6c_%=\n\t" \
    "s_branch conv_b6c_%=\n\t" \
    "conv_w6t_%=:\n\t" \
    "s_memrealtime s[80:81]\n\t" \
    "s_waitcnt lgkmcnt(0)\n\t" \
    "s_sub_u32 s80, s80, %[tg]\n\t" \
    "s_cmp_lt_i32 s80, %[lag]\n\t" \
    "s_cbranch_scc1 conv_w6t_%=\n\t" \
    "s_branch conv_b6t_%=\n\t" \
    "conv_tr_%=:\n\t" \
    "s_waitcnt lgkmcnt(0)\n\t" \
    "v_mfma_f32_16x16x32_bf16 v[96:99], v[248:251], v[196:199], v[96:99]\n\t" \
    "s_cmp_lt_u32 s80, %[tg]\n\t" \
    "s_cbranch_scc1 conv_w6t_%=\n\t" \
    "conv_b6t_%=:\n\t" \
    "v_mfma_f32_16x16x32_bf16 v[100:103], v[248:251], v[204:207], v[100:103]\n\t" \
    "v_mfma_f32_16x16x32_bf16 v[100:103], v[240:243], v[196:199], v[100:103]\n\t" \
    "v_mfma_f32_16x16x32_bf16 v[104:107], v[248:251], v[212:215], v[104:107]\n\t" \
    "v_mfma_f32_16x16x32_bf16 v[104:107], v[240:243], v[204:207], v[104:107]\n\t" \
    "v_mfma_f32_16x16x32_bf16 v[104:107], v[232:235], v[196:199], v[104:107]\n\t" \
    "v_mfma_f32_16x16x32_bf16 v[108:111], v[248:251], v[220:223], v[108:111]\n\t" \
    "v_perm_b32 v96, v97, v96, %[sel]\n\t" \
    "v_perm_b32 v97, v99, v98, %[sel]\n\t" \
    "v_mfma_f32_16x16x32_bf16 v[108:111], v[240:243], v[212:215], v[108:111]\n\t" \
    "ds_write_b64 %[yb], v[96:97] offset:0\n\t" \
    "v_perm_b32 v100, v101, v100, %[sel]\n\t" \
    "v_mfma_f32_16x16x32_bf16 v[108:111], v[232:235], v[204:207], v[108:111]\n\t" \
    "v_perm_b32 v101, v103, v102, %[sel]\n\t" \
    "ds_write_b64 %[yb], v[100:101] offset:512\n\t" \
    "v_mfma_f32_16x16x32_bf16 v[108:111], v[224:227], v[196:199], v[108:111]\n\t" \
    "v_perm_b32 v104, v105, v104, %[sel]\n\t" \
    "v_perm_b32 v105, v107, v106, %[sel]\n\t" \
    "v_mfma_f32_16x16x32_bf16 v[112:115], v[248:251], v[216:219], v[112:115]\n\t" \
    "ds_write_b64 %[yb], v[104:105] offset:1024\n\t" \
    "v_mfma_f32_16x16x32_bf16 v[112:115], v[240:243], v[220:223], v[112:115]\n\t" \
    "v_mfma_f32_16x16x32_bf16 v[112:115], v[232:235], v[212:215], v[112:115]\n\t" \
    "v_mfma_f32_16x16x32_bf16 v[112:115], v[224:227], v[204:207], v[112:115]\n\t" \
    "v_mfma_f32_16x16x32_bf16 v[112:115], v[228:231], v[196:199], v[112:115]\n\t" \
    "v_perm_b32 v108, v109, v108, %[sel]\n\t" \
    "v_perm_b32 v109, v111, v110, %[sel]\n\t" \
    "v_mfma_f32_16x16x32_bf16 v[116:119], v[248:251], v[208:211], v[116:119]\n\t" \
    "ds_write_b64 %[yb], v[108:109] offset:1536\n\t" \
    "v_mfma_f32_16x16x32_bf16 v[116:119], v[240:243], v[216:219], v[116:119]\n\t" \
    "v_mfma_f32_16x16x32_bf16 v[116:119], v[232:235], v[220:223], v[116:119]\n\t" \
    "ds_write_b32 %[fa], %[fa]\n\t" \
    "v_mfma_f32_16x16x32_bf16 v[116:119], v[224:227], v[212:215], v[116:119]\n\t" \
    "v_perm_b32 v112, v113, v112, %[sel]\n\t" \
    "v_perm_b32 v113, v115, v114, %[sel]\n\t" \
    "v_mfma_f32_16x16x32_bf16 v[116:119], v[228:231], v[204:207], v[116:119]\n\t" \
    "ds_write_b64 %[yb], v[112:113] offset:2048\n\t" \
    "v_mfma_f32_16x16x32_bf16 v[116:119], v[236:239], v[196:199], v[116:119]\n\t" \
    "v_mfma_f32_16x16x32_bf16 v[120:123], v[248:251], v[200:203], v[120:123]\n\t" \
    "v_mfma_f32_16x16x32_bf16 v[120:123], v[240:243], v[208:211], v[120:123]\n\t" \
    "v_mfma_f32_16x16x32_bf16 v[120:123], v[232:235], v[216:219], v[120:123]\n\t" \
    "v_mfma_f32_16x16x32_bf16 v[120:123], v[224:227], v[220:223], v[120:123]\n\t" \
    "v_mfma_f32_16x16x32_bf16 v[120:123], v[228:231], v[212:215], v[120:123]\n\t" \
    "v_mfma_f32_16x16x32_bf16 v[120:123], v[236:239], v[204:207], v[120:123]\n\t" \
    "v_mfma_f32_16x16x32_bf16 v[120:123], v[244:247], v[196:199], v[120:123]\n\t" \
    "v_mfma_f32_16x16x32_bf16 v[124:127], v[248:251], v[192:195], v[124:127]\n\t" \
    "v_perm_b32 v116, v117, v116, %[sel]\n\t" \
    "v_perm_b32 v117, v119, v118, %[sel]\n\t" \
    "v_mfma_f32_16x16x32_bf16 v[124:127], v[240:243], v[200:203], v[124:127]\n\t" \
    "ds_write_b64 %[yb], v[116:117] offset:2560\n\t" \
    "v_mfma_f32_16x16x32_bf16 v[124:127], v[232:235], v[208:211], v[124:127]\n\t" \
    "v_mfma_f32_16x16x32_bf16 v[124:127], v[224:227], v[216:219], v[124:127]\n\t" \
    "v_mfma_f32_16x16x32_bf16 v[124:127], v[228:231], v[220:223], v[124:127]\n\t" \
    "v_perm_b32 v120, v121, v120, %[sel]\n\t" \
    "v_perm_b32 v121, v123, v122, %[sel]\n\t" \
    "v_mfma_f32_16x16x32_bf16 v[124:127], v[236:239], v[212:215], v[124:127]\n\t" \
    "ds_write_b64 %[yb], v[120:121] offset:3072\n\t" \
    "v_mfma_f32_16x16x32_bf16 v[124:127], v[244:247], v[204:207], v[124:127]\n\t" \
    "v_mfma_f32_16x16x32_bf16 v[124:127], v[252:255], v[196:199], v[124:127]\n\t" \
    "s_nop 7\n\t" \
    "v_perm_b32 v124, v125, v124, %[sel]\n\t" \
    "v_perm_b32 v125, v127, v126, %[sel]\n\t" \
    "ds_write_b64 %[yb], v[124:125] offset:3584\n\t" \
    "conv_end_%=:\n\t"
#define CONV_CLOB \
    "s80", "s81", "v96", "v97", "v98", "v99", "v100", "v101", "v102", "v103", "v104", "v105", "v106", "v107", "v108", "v109", "v110", "v111", \
    "v112", "v113", "v114", "v115", "v116", "v117", "v118", "v119", "v120", "v121", "v122", "v123", "v124", "v125", "v126", "v127", "v128", \
    "v129", "v130", "v131", "v132", "v133", "v134", "v135", "v136", "v137", "v138", "v139", "v140", "v141", "v142", "v143", "v144", "v145", \
    "v146", "v147", "v148", "v149", "v150", "v151", "v152", "v153", "v154", "v155", "v156", "v157", "v158", "v159", "v160", "v161", "v162", \
    "v163", "v164", "v165", "v166", "v167", "v168", "v169", "v170", "v171", "v172", "v173", "v174", "v175", "v176", "v177", "v178", "v179", \
    "v180", "v181", "v182", "v183", "v184", "v185", "v186", "v187", "v188", "v189", "v190", "v191", "v192", "v193", "v194", "v195", "v196", \
    "v197", "v198", "v199", "v200", "v201", "v202", "v203", "v204", "v205", "v206", "v207", "v208", "v209", "v210", "v211", "v212", "v213", \
    "v214", "v215", "v216", "v217", "v218", "v219", "v220", "v221", "v222", "v223", "v224", "v225", "v226", "v227", "v228", "v229", "v230", \
    "v231", "v232", "v233", "v234", "v235", "v236", "v237", "v238", "v239", "v240", "v241", "v242", "v243", "v244", "v245", "v246", "v247", \
    "v248", "v249", "v250", "v251", "v252", "v253", "v254", "v255"

#define PRIO
#define TILE 10
#define T3 832
#define YG3 T3
#define YG2 (T3 - T3Q)
#define YG1 (T3 - 2 * T3Q)
#define YG0 (T3 - 3 * T3Q)
#define YSP 2
#define TY 792
#define TH 0
#define T3L 20
#define DLY3 20
#define OUTM 2
#define TF 592
#define PFM 1
#define WDP 6
#define QUIET 0
#define TQ 0
#define T2 412
#define PACE 4
#define WSY 32
#define PAUX 0
#define DAUX 0
#define P2 0
#define PAUX2 16
#define DLYO 0
#define WE 3
#define WT WE
#define WSER 0
#define TW 115
#define PACES 0x8866
#define PACEW 0
#define EPOS 0
#define TQ0 0
#define QPOS 2
#define WD0 0
#define EQ0 0
#define WDSYN 32
#define SENTP 1
#define T1 262
#define T1S 0
#define HPOS 11
#define WAUX 0
#define KAUX 2
#define HAUX 2
#define QNT 0
#define GNT 0
#define OAUX 16
template <int NTP, class T> __device__ __forceinline__ T ldg(const GAS void* p) {
  return NTP ? __builtin_nontemporal_load((const GAS T*)p) : *(const GAS T*)p;
}
#define DLY1 0
#define DLY2 0
#define DLY4 26
#define X1A 16
#define X2A 16
#define X1S 16
#define X2S 16
#define TD 0
#define DAUX2 DAUX
#define FAST 0
#define FSY 32
#define TW3 0
#define EXQ 0
#define EXG 0
#define TG0 560
#define TG1 0
#define TG2 0
#define TG3 0
#define FPOS 3
#define FSH 14
#define NPOS 2
#define XPF 0
#define XPA 16
#define X4L 1
#define WD_EARLY 0
#define CB() asm volatile("" ::: "memory")

__global__ void __launch_bounds__(NT, 2)
k_all(const void* a0, const void* a1, const void* a2, const void* a3, const void* a4, const void* a5, const void* a6,
      const void* a7, const void* a8, const void* a9, const void* a10, const void* a11) {
  u64x4 ka, kb, kc;
  const u64 kp = (u64)__builtin_amdgcn_kernarg_segment_ptr();
  {
    asm volatile("s_load_dwordx8 %0, %3, 0x0\n\ts_load_dwordx8 %1, %3, 0x20\n\ts_load_dwordx8 %2, %3, 0x40"
                 : "=&s"(ka), "=&s"(kb), "=&s"(kc) : "s"(kp));
  }
  SB();
  __shared__ __attribute__((aligned(16))) unsigned char smem[L_END + L_EXTRA];
  LAS unsigned char* lds = (LAS unsigned char*)smem;
  const int tid = threadIdx.x, lane = tid & 63, b = blockIdx.x;
  const int w = __builtin_amdgcn_readfirstlane(tid >> 6);
  const int cb = (b & 7) * 32 + ((b >> 2) & 30) + (b >> 7);
  const int wc = (b >> 7) * 8 + w;
  const int wg16 = (b & 7) * 16 + ((b >> 3) & 15);
  const int rg = lane >> 5;
  const int ch0 = 64 * wc + 2 * (lane & 31);
  const int t0 = 16 * wg16 + 8 * rg;
  u32 kvoff = (u32)(t0 * D + ch0) * 2u;
  u32 khoff = t0 == 0 ? 0x80000000u : kvoff - 3u * 2048u;
  u32 skoff = ch0 * 8;
  u32 pf;
  {
    u64 pc; u32 len;
    asm volatile("s_getpc_b64 %0\n.Lk_all_pf:\n\ts_mov_b32 %1, .Lk_all_end-.Lk_all_pf" : "=s"(pc), "=s"(len));
    pf = ld32<0>(mk((const void*)pc, len), ((u32)tid * 8u + ((b >> 3) & 7)) * 128u);
  }
  KEEP(kvoff); KEEP(khoff); KEEP(skoff);
  SB();
  asm volatile("s_waitcnt lgkmcnt(0)" : "+s"(ka), "+s"(kb), "+s"(kc));
  SB();
  u64 ts0_ = __builtin_amdgcn_s_memrealtime();
  SB();
  const u16 *k = (const u16*)ka[0], *v = (const u16*)ka[1], *shift_k = (const u16*)ka[2], *ssm_k = (const u16*)ka[3];
  const rsrc_t rk = mk(k, S * D * 2), rv = mk(v, S * D * 2);
  u32 kr[11], vr[8];
#pragma unroll
  for (int i = 0; i < 3; ++i) kr[i] = ld32<KAUX>(rk, khoff, i * 2048);
  const u32x4 skw = *(const GAS u32x4*)((const GAS char*)shift_k + skoff);
#pragma unroll
  for (int i = 0; i < 8; ++i) kr[3 + i] = ld32<KAUX>(rk, kvoff, i * 2048);
#pragma unroll
  for (int i = 0; i < 8; ++i) vr[i] = ld32<KAUX>(rv, kvoff, i * 2048);
  SB();
  unsigned char* ws = (unsigned char*)kb[0];
  const u16 *ssm_D = (const u16*)kb[1], *q = (const u16*)kb[2];
  const u64 eold = *(const CAS u64*)(ws + WS_E);
  const u64 dall = *(const CAS u64*)(ssm_D + cb * 4);
  const u64 hmid = *(const CAS u64*)(ssm_k + (cb * 4 + (w & 3)) * S + 1024);
  SB();
  u32 tpf = 0;
  if (TLBP) {
    const u32 j = b >> 3;
    const u64 a7 = (j < 16 ? (j < 8 ? kc[0] : kc[1]) : (j < 24 ? kc[2] : kc[3])), a6 = (j & 4) ? kb[3] : kb[2];
    const u32 o7 = umin((j & 7) << 20, (j < 24 ? D * F * 2 : S * D * 2) - 128), o6 = (j & 4) ? 0u : (j & 3) * 0x155500u;
    tpf = ld32<0>(mk((const void*)(w == 7 ? a7 : a6), w == 7 || (w == 6 && j < 8) ? 0x7fffffffu : 0u), 0, w == 7 ? o7 : o6);
    SB();
  }
  STAMP_INIT();
  const rsrc_t rws = mk(ws, WS_END);
  if (CB2 && tid == 0 && b < 8) st32<SC1>((u32)ts0_, rws, WS_TB + b * 4);
  SB();
  const rsrc_t rh = mk(ssm_k, S * D * 2);
  u32x4 ha[2];
  const int hp0 = (w >> 2) * 128 + lane;
  {
    const int z = tid & 31;
    if (tid < 160 && z < 30) LDS128(tid < 128 ? L_XP + (tid >> 5) * XP_CH + z * 16 : L_KEY + z * 16) = u32x4{0, 0, 0, 0};
    if (tid >= 480) LDS128(L_HC + ((tid >> 1) & 15) * HC_CP + (tid & 1) * 16) = u32x4{0, 0, 0, 0};
  }
  u64 tag, mix, kwr = kb[3];
  u32 wm0, wm1, xpf = 0;
  u32 xoff = WS_X + cb * XS + w * 2048 + ((lane & 15) >> 1) * 128 + ((lane >> 4) & 1) * 64 + (lane & 1) * 32 + (lane >> 5) * 16;
  u32 xcoff = WS_CK + ((cb >> 4) * 128 + w * 16 + ((lane & 31) >> 1)) * 512 + (cb & 15) * 32 + (lane & 1) * 16 + (lane >> 5) * 8;
  u32 soff = WS_CAND + ((b & 7) * 16 + ((b >> 3) & 15)) * 4096 + (tid & 255) * 16;
  u32 f2o = lane == 0 && b < 128 ? WS_F2 + wg16 * 128 + w * 8 : 0x80000000u;
  {
    u32 off = WS_X + (16 * wc + ((lane & 15) >> 1)) * XS + wg16 * 128 + ((lane >> 4) & 1) * 64 + rg * 32 + (lane & 1) * 16;
    u32 off2 = off + 8 * XS;
    u32 coff = (lane & 1) ? 0x80000000u : WS_CK + (wc * 128 + wg16) * 512 + (lane & 30) * 16 + rg * 16;
    u32 hoff = (u32)((cb * 4 + (w & 3)) * S) * 2u + hp0 * 16;
    KEEP(off); KEEP(off2); KEEP(coff); KEEP(hoff); KEEP(xoff); KEEP(xcoff); KEEP(soff); KEEP(f2o);
    SB();
    if (HPOS < 0) {
#pragma unroll
      for (int i = 0; i < 2; ++i) ha[i] = ld128<HAUX>(rh, hoff, i * 1024);
      SB();
    }
    const f32x2 sk[4] = {{bflo(skw.x), bflo(skw.z)}, {bfhi(skw.x), bfhi(skw.z)}, {bflo(skw.y), bflo(skw.w)}, {bfhi(skw.y), bfhi(skw.w)}};
    f32x2 kf[11], a[8];
#pragma unroll
    for (int i = 0; i < HPOS; ++i) { kf[i] = f32x2{bflo(kr[i]), bfhi(kr[i])}; KEEP(kf[i]); }
    SB();
    if (HPOS >= 0) {
#pragma unroll
      for (int i = 0; i < 2; ++i) ha[i] = ld128<HAUX>(rh, hoff, i * 1024);
      SB();
    }
#pragma unroll
    for (int i = HPOS < 0 ? 0 : HPOS; i < 11; ++i) kf[i] = f32x2{bflo(kr[i]), bfhi(kr[i])};
#pragma unroll
    for (int i = 0; i < 8; ++i) {
      a[i] = kf[i] * sk[3];
#pragma unroll
      for (int j = 1; j < 4; ++j) a[i] = __builtin_elementwise_fma(kf[i + j], sk[3 - j], a[i]);
      KEEP(a[i]);
    }
    SB();
    tag = eold + 1ull;
    mix = tag * 0x9E3779B97F4A7C15ull;
    wm0 = (u32)(mix >> 17) & ~1u; wm1 = (u32)mix * 0x85EBCA77u;
    asm volatile("" :: "s"(wm0), "s"(wm1), "s"(mix));
    SB();
#pragma unroll
    for (int i = 0; i < 8; ++i) { a[i] *= f32x2{bflo(vr[i]), bfhi(vr[i])}; KEEP(a[i]); }
    u32x4 p[2];
#pragma unroll
    for (int c = 0; c < 2; ++c)
      p[c] = u32x4{(pack2(reven(a[7][c]), a[6][c]) & ~1u) ^ wm0, pack2(a[5][c], a[4][c]) ^ wm1,
                   (pack2(reven(a[3][c]), a[2][c]) & ~1u) ^ wm0, pack2(a[1][c], a[0][c]) ^ wm1};
    const u32x2 ck = check(p[0], p[1], mix);
    st128<SC1>(u32x4{ck.x, ck.y, dppu<XOR1>(ck.x), dppu<XOR1>(ck.y)}, rws, coff);
    SB();
#pragma unroll
    for (int i = 0; i < 4; ++i) { auto r = __builtin_amdgcn_permlane16_swap(p[0][i], p[1][i], false, false); p[0][i] = r[0]; p[1][i] = r[1]; }
    st128<X1S>(p[0], rws, off);
    st128<X1S>(p[1], rws, off2);
    SB();
    if (w < 4 && b >= 128) st128<SC1>(u32x4{SENT, SENT, SENT, SENT}, rws, soff);
    if (tid < KST / 2 && b < 16 * NREP) st128<SC1>(u32x4{SENT, SENT, SENT, SENT}, rws, WS_KEY + (b >> 4) * KPG + (b & 15) * (8 * KST) + tid * 16);
    SB();
    STAMP(1); STAMPW(24, 7);
  }
  SB();
  KEEP(pf); KEEP(tpf);
  auto tapcopy = [&]() __attribute__((always_inline)) {
    const int base = L_HC + (w & 3) * HC_CH + 32 + hp0 * 16;
    if (CSEQ != 11) {
      const u32 t0d = pack2(bflo(ha[0].x) + bflo((u32)(dall >> ((w & 3) * 16))), bfhi(ha[0].x));
      ha[0].x = hp0 == 0 ? t0d : ha[0].x;
    }
#pragma unroll
    for (int i = 0; i < 2; ++i) {
      const u32 n0 = i == 0 ? __builtin_amdgcn_readfirstlane(ha[1].x) : (w < 4 ? (u32)hmid : 0u);
      const u32 n1 = i == 0 ? __builtin_amdgcn_readfirstlane(ha[1].y) : (w < 4 ? (u32)(hmid >> 32) : 0u);
      const u32 d[6] = {ha[i].x, ha[i].y, ha[i].z, ha[i].w, (u32)__builtin_amdgcn_update_dpp((int)n0, (int)ha[i].x, 0x130, 0xf, 0xf, false),
                        (u32)__builtin_amdgcn_update_dpp((int)n1, (int)ha[i].y, 0x130, 0xf, 0xf, false)};
      u32 e[5];
#pragma unroll
      for (int j = 0; j < 5; ++j) e[j] = __builtin_amdgcn_alignbit(d[j + 1], d[j], 16);
      LDS128(base + i * 1024) = ha[i];
      LDS128(base + i * 1024 + HC_CP) = u32x4{e[0], e[1], e[2], e[3]};
      LDS128(base + i * 1024 + 2 * HC_CP) = u32x4{d[1], d[2], d[3], d[4]};
      LDS128(base + i * 1024 + 3 * HC_CP) = u32x4{e[1], e[2], e[3], e[4]};
      if (i == 0 && hp0 == 0) {
        LDS32(base - 4 + HC_CP) = d[0] << 16;
        LDS32(base - 4 + 2 * HC_CP) = d[0];
        LDS64(base - 8 + 3 * HC_CP) = u32x2{d[0] << 16, e[0]};
      }
    }
  };
  u32 tb = (u32)ts0_;
  if (CB2 && w == 0) {
    while ((u32)__builtin_amdgcn_s_memrealtime() - tb < TR) {}
    u64x4 tbs;
    asm volatile("s_load_dwordx8 %0, %1, %2\n\ts_waitcnt lgkmcnt(0)" : "=&s"(tbs) : "s"(ws), "n"(WS_TB));
    const u32 c = tb - 128u;
    u32 mn = 128u, mx = 127u;
#pragma unroll
    for (int i = 0; i < 8; ++i) {
      const u32 d = (u32)(tbs[i >> 1] >> (32 * (i & 1))) - c;
      mn = umin(mn, d); asm volatile("" : "+s"(mn));
      mx = umin(mx, 255u - d); asm volatile("" : "+s"(mx));
    }
    LDS32(L_SYN + 440) = c + TOFF + umax(mn, 255u - CL - mx);
    SB();
  }
  const u16 *w_r = (const u16*)kwr, *wg = (const u16*)kc[0], *wu = (const u16*)kc[1], *wd = (const u16*)kc[2];
  u16* out = (u16*)kc[3];
#define NOW() ((u32)__builtin_amdgcn_s_memrealtime() - tb)
  auto recv = [&](auto pre, auto extra) __attribute__((always_inline)) {
    BAR();
    if (CB2) tb = __builtin_amdgcn_readfirstlane(*(volatile LAS u32*)(lds + L_SYN + 440));
    STAMP(22);
    while (NOW() < T1 + T1S * ((cb >> 4) & 7)) {}
    STAMP(4);
    pre();
    u32x4 xin[2];
#pragma unroll
    for (int i = 0; i < 2; ++i) xin[i] = ld128<X1A>(rws, xoff, i * 1024);
    u32x2 ck = ld64<X1A>(rws, xcoff);
    SB();
    tapcopy();
    SB();
    extra();
    SB();
    asm volatile("s_waitcnt vmcnt(3)" ::: "memory");
    st64<SC1>(u32x2{(u32)tag, (u32)(tag >> 32)}, rws, f2o);
    SB();
    STAMP(2);
    for (int spins = 0; spins < SPIN_CAP; ++spins) {
#pragma unroll
      for (int i = 0; i < 4; ++i) { auto r = __builtin_amdgcn_permlane16_swap(xin[0][i], xin[1][i], false, false); xin[0][i] = r[0]; xin[1][i] = r[1]; }
#pragma unroll
      for (int i = 0; i < 2; ++i)
        LDS128(L_XP + (2 * (lane >> 5) + i) * XP_CH + (30 + 32 * w + (lane & 31)) * 16) = xin[i] ^ u32x4{wm0, wm1, wm0, wm1};
      SB();
      const u32x2 c0 = check(xin[0], xin[1], mix);
      if (__all(((c0.x ^ ck.x) | (c0.y ^ ck.y)) == 0u)) break;
      CB();
#pragma unroll
      for (int i = 0; i < 2; ++i) xin[i] = ld128<X1A>(rws, xoff, i * 1024);
      ck = ld64<X1A>(rws, xcoff);
      asm volatile("s_waitcnt vmcnt(0)" : "+v"(xin[0]), "+v"(xin[1]), "+v"(ck) :: "memory");
    }
    STAMP(23);
  };
  u32 ck0 = 0, ct0 = 0;
  auto clk0 = [&]() __attribute__((always_inline)) {
    u64 c, t;
    asm volatile("s_memtime %0\n\ts_memrealtime %1\n\ts_waitcnt lgkmcnt(0)" : "=&s"(c), "=&s"(t));
    ck0 = (u32)c; ct0 = (u32)t;
  };
  auto clk1 = [&](float n, u32 cap) __attribute__((always_inline)) {
    u64 c, t;
    asm volatile("s_memtime %0\n\ts_memrealtime %1\n\ts_waitcnt lgkmcnt(0)" : "=&s"(c), "=&s"(t));
    const float r = (float)((u32)t - ct0) * __builtin_amdgcn_rcpf((float)((u32)c - ck0));
    u32 x = (u32)fmaxf((r - CKR * 1e-4f) * n, 0.f);
    KEEP(x);
    return umin(__builtin_amdgcn_readfirstlane(x), cap);
  };
  const int fj = b >> 3;
  const rsrc_t ro = mk(out, S * D * 2);
  u32x2 fl;
  auto flags0 = [&]() __attribute__((always_inline)) {
    while (NOW() < TF) __builtin_amdgcn_s_sleep(1);
    CB();
    asm volatile("buffer_load_dwordx2 v[78:79], %1, %2, 0 offen sc1" : "=&{v[78:79]}"(fl) : "v"(WS_FLAG + (tid - 256) * 8), "s"(rws) : "memory");
    SB();
  };
  auto flags1 = [&](auto n) __attribute__((always_inline)) {
    asm volatile("s_waitcnt vmcnt(%1)" : "+{v[78:79]}"(fl) : "n"(n()) : "memory");
#pragma unroll 1
    for (int spins = 0; spins < SPIN_CAP; ++spins) {
      if (__all(((fl.x ^ (u32)tag) | (fl.y ^ (u32)(tag >> 32))) == 0u)) break;
      CB();
      fl = ld64<SC1>(rws, WS_FLAG + (tid - 256) * 8);
      asm volatile("s_waitcnt vmcnt(0)" : "+v"(fl) :: "memory");
    }
    LDS32(L_SYN + w * 4) = 1u;
    STAMPW(21, 7);
    SB();
  };
  auto waitsyn = [&](int o) __attribute__((always_inline)) {
    for (;;) {
      CB();
      const u32x4 f = *(volatile LAS u32x4*)(lds + L_SYN + o);
      if (__builtin_amdgcn_readfirstlane(f.x & f.y & f.z & f.w)) break;
      __builtin_amdgcn_s_sleep(1);
    }
    SB();
  };
  auto stripa = [&](u32 id) __attribute__((always_inline)) {
    const u32 r = (id >> 1) & 255u;
    const u32 o = ((r >> 6) * 256 + (id >> 9) * 64 + (r & 63u)) * (F * 2) + (b & 7) * 896 + umin(lane & 7, 6) * 128;
    u64 x = (u64)((id & 1u) ? wu : wg) + o;
    KEEP(x);
    return x;
  };
  auto strip = [&](u64 a) __attribute__((always_inline)) {
    const u32 x = *(const GAS u32*)a;
    SB();
    return x;
  };
  auto wpe = [&]() __attribute__((always_inline)) {};
  auto top1 = [&](u32 cur, int own, u32 t) __attribute__((always_inline)) {
    u32x4 xs[2];
    u32 koff = WS_KEY + (b & (NREP - 1)) * KPG + lane * KST, xso = WS_CAND + lane * 16, xsa = L_XS + lane * 16;
    KEEP(koff); KEEP(xso); KEEP(xsa);
    SB();
    while (NOW() < t) {}
    if (CKC && own < 0) {
      const u32 c0 = (u32)__builtin_amdgcn_s_memtime();
      while ((u32)__builtin_amdgcn_s_memtime() - c0 < CKC) {}
    }
    STAMPY(24);
    u32 kk[2], tt[2] = {0, 0};
    auto ldk = [&](int i) __attribute__((always_inline)) {
      kk[i] = ld32<SC1>(rws, koff, i * 64 * KST);
      if (KTD && own >= 0) tt[i] = ld32<SC1>(rws, WS_KEY + 2048 + (b & (TREP - 1)) * 4096 + lane * 16, i * 1024);
    };
    CB();
#pragma unroll
    for (int i = 0; i < 2; ++i) ldk(i);
    SB();
#pragma unroll
    for (int i = 0; i < 2; ++i) if (own >= 0) { kk[i] = lane + 64 * i == own ? cur : kk[i]; tt[i] = lane + 64 * i == own ? tb : tt[i]; }
    for (int spins = 0; spins < SPIN_CAP; ++spins) {
      u32 m = umax(kk[0] + 1u, kk[1] + 1u);
      m = umax(m, dppu<XOR1>(m)); m = umax(m, dppu<XOR2>(m)); m = umax(m, dppu<XOR7>(m)); m = umax(m, dppu<XOR8>(m));
      u32 c0 = umax(__builtin_amdgcn_readlane(m, 0), __builtin_amdgcn_readlane(m, 16)), c1 = umax(__builtin_amdgcn_readlane(m, 32), __builtin_amdgcn_readlane(m, 48));
      asm volatile("" : "+s"(c0), "+s"(c1));
      c1 = umax(c0, c1);
      cur = c1 - 1u;
      if (own >= 0 && !__builtin_amdgcn_ballot_w64(umax(kk[0], kk[1]) >= c1)) break;
      CB();
      const u32 cblk = (2047u - (cur & 2047u)) >> 4;
      if (own < 0) {
#pragma unroll
        for (int i = 0; i < 2; ++i) xs[i] = ld128<SC1>(rws, xso, cblk * 4096 + i * 1024);
      }
      const u32 miss = __builtin_popcountll(__builtin_amdgcn_ballot_w64(kk[0] == SENT)) + __builtin_popcountll(__builtin_amdgcn_ballot_w64(kk[1] == SENT));
#pragma unroll
      for (int i = 0; i < 2; ++i)
        if (kk[i] == SENT) ldk(i);
      SB();
      if (own < 0) {
        if (spins == 0) { STAMPY(22); }
        if (!TD2) { LDS32(L_KEY + 36) = RMS < 128 ? miss <= RMS : 1u; SB(); }
#pragma unroll
        for (int i = 0; i < 2; ++i) LDS128(xsa + i * 1024) = xs[i];
        SB();
        if (!(__builtin_amdgcn_ballot_w64(umax(kk[0], kk[1]) >= c1) | __builtin_amdgcn_ballot_w64(umax(vmax4(xs[0]), vmax4(xs[1])) == SENT))) break;
      }
    }
    if (KTD && own >= 0) {
      const u32 tm = umin(__builtin_amdgcn_readfirstlane(wave_umax(umax(tt[0] - tb, tt[1] - tb))), NOW()) + KTD;
      while (NOW() < tm) __builtin_amdgcn_s_sleep(1);
    }
    CB();
    if (RMS < 128) LDS64(L_KEY + 32) = u32x2{cur | 0x8000u, 1u}; else
    LDS32(L_KEY + 32) = cur | 0x8000u;
    STAMPY(23);
  };
  if (w >= 4 && fj < NFX) {
    const int hw = w - 4, g = lane >> 4;
    const int fb = (b & 7) * NFX + fj;
    u32x4 w1[4][8];
    const rsrc_t rd = mk(wd, D * F * 2);
    const u32 doff = (u32)((fb * 32 + g * 8) * D + hw * 256) * 2u + (lane & 15) * 8;
    const GAS unsigned char* p = (const GAS unsigned char*)((lane & 4) ? wu : wg) +
                                 (size_t)(hw * 256 + ((lane >> 3) & 1) * 32 + (g >> 1) * 16 + (g & 1) * 4) * (F * 2) + fb * 64 + (lane & 3) * 16;
    u32 fast_ = 0;
#define DBGT(st, j)
#define LDQ(st) do { \
      _Pragma("unroll") for (int j = 0; j < 8; ++j) { \
        SB(); asm volatile("s_waitcnt vmcnt(%0)" :: "n"(((PACES >> (4 * (st))) & 15) - 1) : "memory"); SB(); \
        if (QEND && (j % QEND) == 0 && (st) > 0) { if (__builtin_amdgcn_readfirstlane(*(volatile LAS u32*)(lds + L_SYN + 112))) waitsyn(QSY); SB(); } \
        w1[st][j] = *(const GAS u32x4*)(p + (size_t)((st) * 64 + (j & 1) + (j & 2) * 4 + (j & 4) / 2) * (F * 2)); } \
      SB(); } while (0)
#define TRQ(st) do { \
      _Pragma("unroll") for (int f = 0; f < 8; ++f) \
        _Pragma("unroll") for (int jp = 0; jp < 4; ++jp) \
          wt[st][f >> 1][(f & 1) * 4 + jp] = __builtin_amdgcn_perm(w1[st][2 * jp + 1][f >> 1], w1[st][2 * jp][f >> 1], (f & 1) ? 0x07060302u : 0x05040100u); \
      _Pragma("unroll") for (int f = 0; f < 4; ++f) KEEP(wt[st][f]); \
      SB(); } while (0)
    u32x8 wt[4][4], w3[4][2];
    auto ldwd = [&](int c0, int c1) __attribute__((always_inline)) {
#pragma unroll
      for (int j = 0; j < 8; ++j)
#pragma unroll
        for (int c = c0; c < c1; ++c) {
          const u32x2 x = ld64<WAUX>(rd, doff + c * 128, j * 2048);
          w3[c][j >> 2][(j & 3) * 2] = x.x; w3[c][j >> 2][(j & 3) * 2 + 1] = x.y;
          SB();
        }
    };
    u64 sa = 0, sb = 0;
    u32 wpf0 = 0, wpf1 = 0;
    constexpr int F0 = FSH > 8 ? 8 : FSH, F1 = FSH - F0;
    if (F0 && !FNO) sa = stripa((fj * 4 + hw) * F0 + umin(lane >> 3, F0 ? F0 - 1 : 0));
    if (F1 && !FNO) sb = stripa(448 + (fj * 4 + hw) * F1 + umin(lane >> 3, F1 ? F1 - 1 : 0));
    recv(wpe, wpe);
    BAR();
    if (tid == 256) __hip_atomic_store((GAS u64*)(ws + WS_FLAG) + b, tag, __ATOMIC_RELAXED, __HIP_MEMORY_SCOPE_AGENT);
    SB();
    if (WDQ < 0) ldwd(0, WE0);
    while (NOW() < T2) __builtin_amdgcn_s_sleep(1);
    SB();
#define GATE(t) do { if (t) { while (NOW() < (t)) __builtin_amdgcn_s_sleep(1); SB(); } } while (0)
    if (F0 && !FNO) wpf0 = strip(sa);
    if (F1 && !FNO && !FOR) wpf1 = strip(sb);
    if (WDQ == 0) ldwd(0, WE0);
    if (F1 && !FNO && FOR) wpf1 = strip(sb);
    GATE(TG0);
    STAMPF(26);
    LDQ(0);
    KEEP(wpf0); KEEP(wpf1);
    if (WDQ == 1) ldwd(0, WE0);
    STAMPF(27);
    GATE(TG1);
    LDQ(1);
    if (WDQ == 2) ldwd(0, WE0);
    STAMPF(28);
    GATE(TG2);
    LDQ(2);
    STAMPF(29);
    GATE(TG3);
    LDQ(3);
    if (QAL) LDS32(L_SYN + 16 + hw * 4) = 1u;
    STAMPF(13);
    if (WF == 0) { TRQ(0); TRQ(1); TRQ(2); TRQ(3); STAMPF(15); }
    if (WF == 2) { TRQ(0); }
    GATE(TD1);
    if (QAL) waitsyn(16);
    ldwd(WE0, WE);
    STAMPF(31);
    if (WF == 1) { TRQ(0); }
    if (WF) { TRQ(1); TRQ(2); TRQ(3); STAMPF(15); }
    u32 ln;
    asm volatile("v_mbcnt_lo_u32_b32 %0, -1, 0\n\tv_mbcnt_hi_u32_b32 %0, -1, %0" : "=v"(ln));
    u32 xaa = ((ln >> 2) & 1) == (ln >> 5) ? L_XS + hw * 512 + (ln & 1) * 64 + ((ln >> 4) & 1) * 32 + (ln & 2) * 8 : L_XP;
    u32 rwa = (ln & 40) ? L_Q + hw * 1024 + ln * 16 : L_RED + hw * 256 + (ln & 7) * 32 + (ln & 16), fwa = L_SYN + 48 + hw * 4, sidx = (ln & 2) ? 0xEEEEu : 0x4444u, amask = (ln & 1) ? 0xffff0000u : 0xffffu;
    const u32 un = (ln & 17) + (ln >> 5) * 2 + (ln & 6) * 2;
    u32 rra = L_RED + (un >> 3) * 32 + (un & 1) * 16 + (un & 6) * 2;
    f32x4 acc[4], ac2[4];
    u32x4 aa[2];
#pragma unroll
    for (int i = 0; i < 4; ++i) { acc[i] = f32x4{0.f, 0.f, 0.f, 0.f}; KEEP(acc[i]); }
#pragma unroll
    for (int i = 0; i < 4; ++i) { ac2[i] = f32x4{0.f, 0.f, 0.f, 0.f}; KEEP(ac2[i]); }
    KEEP(sidx); KEEP(amask); KEEP(fwa);
    u32 pso = WS_X + ((b & 7) * 32 + 2 * fj + (hw >> 1)) * XS + (b & 7) * 2048 + (hw & 1) * 1024 + ln * 16;
    KEEP(xaa); KEEP(rwa); KEEP(rra); KEEP(pso);
    STAMPF(30);
    if (WE == 4) {} else if (TD2) { GATE(TD2); } else while (!__builtin_amdgcn_readfirstlane(*(volatile LAS u32*)(lds + L_KEY + 36))) __builtin_amdgcn_s_sleep(1);
    SB();
    ldwd(WE, 4);
    STAMPF(16);
    SB();
    {
      u32x4 xa[4];
      for (;;) {
        CB();
        const u32 f = *(volatile LAS u32*)(lds + L_KEY + 32);
        CB();
#pragma unroll
        for (int st = 0; st < 4; ++st) xa[st] = LDS128(xaa + st * 128);
        CB();
        if (__builtin_amdgcn_readfirstlane(f)) break;
      }
      STAMPF(10);
#pragma unroll
      for (int st = 0; st < 4; ++st)
#pragma unroll
        for (int f = 0; f < 4; ++f) acc[f] = smfma(xa[st], wt[st][f], acc[f], sidx);
      SB();
      f32x4 o;
      float t0[4], t1[4];
#pragma unroll
      for (int f = 0; f < 4; ++f) { t1[f] = acc[f][1] + acc[f][3]; KEEP(t1[f]); t0[f] = acc[f][0] + acc[f][2]; KEEP(t0[f]); SB(); }
#pragma unroll
      for (int f = 0; f < 4; ++f) { float x = dppf<0x108>(t1[f]) + t0[f]; KEEP(x); o[f] = x; }
      *(LAS f32x4*)(lds + rwa) = o;
      CB();
      LDS32(fwa) = sidx;
    }
    SB();
    f32x2 gu[4];
    for (;;) {
      CB();
      const u32x4 f = *(volatile LAS u32x4*)(lds + L_SYN + 48);
      CB();
#pragma unroll
      for (int i = 0; i < 4; ++i) gu[i] = f32x2{*(LAS float*)(lds + rra + i * 256), *(LAS float*)(lds + rra + i * 256 + 128)};
      CB();
      if (__builtin_amdgcn_readfirstlane(f.x & f.y & f.z & f.w)) break;
    }
    const u32 gp = pk((gu[0] + gu[1]) + (gu[2] + gu[3]));
    const float gs = bflo(gp), us = bfhi(gp);
    const float act = rbf(gs * __builtin_amdgcn_rcpf(1.f + __expf(-gs))) * us;
    u32 ap = pack2(act, act);
    KEEP(ap);
    SB();
#define AROW(e, i) aa[e][i] = dppu<0x150 + (i >> 1) * 4 + e * 2 + (i & 1)>(ap) & amask
#define DOWN(c, e) ac2[c] = smfma(aa[e], w3[c][e], ac2[c], sidx); SB()
#define PICK(c, i) ou[i] = (u32)__builtin_amdgcn_update_dpp((int)ou[i], __builtin_bit_cast(int, t##c##i), 0xE4, 1 << c, 0xf, false)
    AROW(0, 0); AROW(0, 1); AROW(0, 2); AROW(0, 3); SB();
    DOWN(0, 0); AROW(1, 0); AROW(1, 1); SB();
    DOWN(1, 0); AROW(1, 2); AROW(1, 3); SB();
    DOWN(2, 0); DOWN(0, 1); DOWN(1, 1); DOWN(2, 1); DOWN(3, 0);
    {
      u32x4 ou = __builtin_bit_cast(u32x4, ac2[0]);
      const float t10 = ac2[1][0], t11 = ac2[1][1], t12 = ac2[1][2], t13 = ac2[1][3], t20 = ac2[2][0], t21 = ac2[2][1], t22 = ac2[2][2], t23 = ac2[2][3];
      DOWN(3, 1);
      PICK(1, 0); PICK(1, 1); PICK(1, 2); PICK(1, 3); SB();
      PICK(2, 0); PICK(2, 1); PICK(2, 2); PICK(2, 3); SB();
      const float t30 = ac2[3][0], t31 = ac2[3][1], t32 = ac2[3][2], t33 = ac2[3][3];
      PICK(3, 0); PICK(3, 2); PICK(3, 1); PICK(3, 3); SB();
      ou.x &= ~1u; ou.z &= ~1u;
      st128<SC1>(ou, rws, pso);
      SB();
    }
    STAMPF(11);
  } else {
    const int ru = lane & 7, rbk = (b & 7) * 16 + (fj & 15);
    const bool rowb = fj >= 16;
    u32x4 qa[2][2], wa[2];
    u32 vw = w * 256 + (lane >> 3) * 16, vq = vw + ((ru >> 2) * 8 + (ru & 3)) * 2048;
    u32 off = WS_X + (u32)(w * 32 + (lane >> 3) * 2) * XS + rbk * 128 + ru * 16;
    u32 ridx = (u32)(2047 - rbk * 16 - (ru >> 2) * 8 - (ru & 3) - (lane >> 5) * 4), kso = lane < NREP * (KST / 16) ? WS_KEY + (lane / (KST / 16)) * KPG + rbk * KST + (lane & (KST / 16 - 1)) * 16 : 0x80000000u;
    u32 rr = L_RW + (ru + (lane >> 5) * 8) * 112, ra = rr + ((lane & 8) ? 64 : (lane & 16) * 2) + w * 4;
    u32 dof = lane < 28 && (WE < 3 || (w & 1)) ? (u32)(rbk * 28 + lane) * 2048u + (w >> 1) * 512 + (w & 1) * 128 + 256 : 0x80000000u;
    if (WP3 >= 3) dof = lane < 56 ? (u32)(rbk * 28 + (lane >> 1)) * 2048u + w * 256 + (lane & 1) * 128 : 0x80000000u;
    KEEP(vw); KEEP(vq); KEEP(off); KEEP(ra); KEEP(rr); KEEP(ridx); KEEP(kso); KEEP(dof);
    auto h32 = [](float u, float v) __attribute__((always_inline)) {
      auto t = __builtin_amdgcn_permlane32_swap(__builtin_bit_cast(u32, u), __builtin_bit_cast(u32, v), false, false);
      return __builtin_bit_cast(float, (u32)t[0]) + __builtin_bit_cast(float, (u32)t[1]); };
    auto h16 = [](float u, float v) __attribute__((always_inline)) {
      auto t = __builtin_amdgcn_permlane16_swap(__builtin_bit_cast(u32, u), __builtin_bit_cast(u32, v), false, false);
      return __builtin_bit_cast(float, (u32)t[0]) + __builtin_bit_cast(float, (u32)t[1]); };
    auto rsum0 = [&](const float* x, const float* y, const float* z, int base) __attribute__((always_inline)) {
      float u = h16(h32(x[0], x[1]), h32(y[0], y[1])), v = h32(z[0], z[1]);
      v = h16(v, v);
      u += dppf<XOR8>(u); v += dppf<XOR8>(v);
      *(LAS float*)(lds + ra + base) = (lane & 8) ? v : u;
      CB();
    };
    auto rsum1 = [&](float& x, float& y, float& z, int base) __attribute__((always_inline)) {
      BAR();
      f32x4 t[6];
#pragma unroll
      for (int i = 0; i < 6; ++i) t[i] = *(LAS f32x4*)(lds + rr + base + i * 16);
      const f32x2 x2 = (t[0].lo + t[0].hi) + (t[1].lo + t[1].hi), y2 = (t[2].lo + t[2].hi) + (t[3].lo + t[3].hi), z2 = (t[4].lo + t[4].hi) + (t[5].lo + t[5].hi);
      x = x2.x + x2.y; y = y2.x + y2.y; z = z2.x + z2.y;
    };
    auto both = [](float x, float* o) __attribute__((always_inline)) {
      auto t = __builtin_amdgcn_permlane32_swap(__builtin_bit_cast(u32, x), __builtin_bit_cast(u32, x), false, false);
      o[0] = __builtin_bit_cast(float, (u32)t[0]); o[1] = __builtin_bit_cast(float, (u32)t[1]);
    };
    auto ldw = [&]() __attribute__((always_inline)) {
#pragma unroll
      for (int i = 0; i < 2; ++i) wa[i] = LDS128(L_XS + vw + i * 128);
    };
    f32x4 s1q[2], s2q[2];
    u32 qla = L_Q + (tid & 255) * 16, cma = L_RED + (w & 3) * 128 + ru * 16, fla = L_SYN + 144 + (w & 3) * 8;
    KEEP(qla); KEEP(cma); KEEP(fla);
    auto qsum = [&](const u32x4 (&x)[2][2], bool first) __attribute__((always_inline)) {
      const u32x4 one = {0x3f803f80u, 0x3f803f80u, 0x3f803f80u, 0x3f803f80u};
#pragma unroll
      for (int h = 0; h < 2; ++h)
#pragma unroll
        for (int r = 0; r < 2; ++r) {
          s2q[r] = mfma(x[r][h], x[r][h], first && h == 0 ? f32x4{0.f, 0.f, 0.f, 0.f} : s2q[r]);
          s1q[r] = mfma(one, x[r][h], first && h == 0 ? f32x4{0.f, 0.f, 0.f, 0.f} : s1q[r]);
        }
    };
    auto ln1a = [&]() __attribute__((always_inline)) {
      qsum(qa, false);
      float d[2];
#pragma unroll
      for (int r = 0; r < 2; ++r) d[r] = (lane & 2) ? ((lane & 1) ? s2q[r][3] : s2q[r][2]) : ((lane & 1) ? s2q[r][1] : s2q[r][0]);
      const u32 pa = L_RW + ru * 112 + (lane & 8) * 2 + (w & 3) * 4;
      *(LAS float*)(lds + pa + (lane >> 5) * 896) = lane < 32 ? s1q[0][0] : s1q[1][0];
      if ((lane >> 4) == (lane & 15) >> 2) { *(LAS float*)(lds + pa + 32) = d[0]; *(LAS float*)(lds + pa + 32 + 896) = d[1]; }
      CB();
      LDS32(L_SYN + 128 + (w & 3) * 4) = 1u;
      SB();
    };
    auto ln1b = [&]() __attribute__((always_inline)) {
      CB();
      f32x4 t0 = *(LAS f32x4*)(lds + rr), t3 = *(LAS f32x4*)(lds + rr + 16), t1 = *(LAS f32x4*)(lds + rr + 32), t4 = *(LAS f32x4*)(lds + rr + 48);
      const f32x4 t2 = *(LAS f32x4*)(lds + L_SYN + 192);
      t0 += t3; t1 += t4;
      const float mean = ((t0.x + t0.y) + (t0.z + t0.w)) * (1.f / D), msq = (t1.x + t1.y) + (t1.z + t1.w);
      const float rs = rstd(fmaxf(fmaf(-mean, mean, msq * (1.f / D)), 0.f));
      *(LAS f32x2*)(lds + cma + (lane >> 5) * 8) = f32x2{rs, -rbf(mean) * rs};
      CB();
      *(LAS f32x2*)(lds + fla) = f32x2{(t2.x + t2.y) + (t2.z + t2.w), 1.f};
      SB();
    };
    constexpr int NN[4] = {NST & 15, (NST >> 4) & 15, (NST >> 8) & 15, (NST >> 12) & 15}, NS = NN[3] ? 4 : NN[2] ? 3 : 2;
    constexpr int RN[4] = {RST & 15, (RST >> 4) & 15, (RST >> 8) & 15, (RST >> 12) & 15};
    static_assert(56 * FSH + 8 * (NN[0] + NN[1] + NN[2] + NN[3]) + 64 * (RN[0] + RN[1] + RN[2] + RN[3]) >= 2048, "strips");
#define QWAIT(n) asm volatile("s_waitcnt vmcnt(%4)" : "+{v[80:83]}"(qa[0][0]), "+{v[84:87]}"(qa[0][1]), "+{v[88:91]}"(qa[1][0]), "+{v[92:95]}"(qa[1][1]) : "n"(n) : "memory")
    u32 wpf0 = 0, wpf1 = 0, wpf4 = 0, wpf5 = 0;
    bool early = false, late = false;
    if (w < 4) {
      u32 ab, bb, yb, ya, yoff = WS_X + cb * XS + w * 4096 + lane * 16;
      {
        const int r = lane & 15, g = lane >> 4, rho = (1 + r) & 3;
        const u32 lb = (u32)(uintptr_t)lds;
        ab = lb + L_HC + w * HC_CH + rho * HC_CP + 2 * (25 + r - 8 * g - rho);
        bb = lb + L_XP + w * XP_CH + 16 * (2 * r + g);
        yb = lb + L_YS + w * YS_CH + ((g & 1) * 32 + (g >> 1) * 16 + r) * 8;
        const int jl = 4 * g + (lane & 3);
        ya = L_YS + ((lane >> 2) & 3) * YS_CH + w * 1024 + (jl & 1) * 128 + (jl >> 1) * 8;
        KEEP(ab); KEEP(bb); KEEP(yb); KEEP(yoff); KEEP(ya);
      }
      SB();
      recv(wpe, wpe);
      __builtin_amdgcn_s_setprio(FPR);
      if (CKK) clk0();
      BAR();
      STAMP(5); STAMPY(5);
      {
        u32 tg = tb - LAG + TC - (NCK - 1) * DT - (((31 - fj) * PHN) >> 3);
        asm volatile(CONV_ASM : [tg] "+s"(tg)
                     : [ab] "v"(ab), [bb] "v"(bb),
                       [yb] "v"(yb), [wv] "s"(w), [sel] "s"(0x07060302u), [dt] "n"(DT), [lag] "n"(LAG),
                       [fa] "v"((u32)(uintptr_t)lds + L_SYN + 112)
                     : "memory", "scc", CONV_CLOB);
        LDS32(L_SYN + w * 4) = 1u;
      }
      if (FPR != 3) __builtin_amdgcn_s_setprio(3);
      STAMP(6); STAMPY(6);
      u32x2 lo[4], hi[4];
      for (;;) {
        CB();
        const u32x4 f = *(volatile LAS u32x4*)(lds + L_SYN);
        CB();
#pragma unroll
        for (int s = 0; s < 4; ++s) {
          lo[s] = __builtin_bit_cast(u32x2, __builtin_amdgcn_ds_read_tr16_b64_v4i16((LAS s16x4*)(lds + ya + (s >> 1) * 512 + (s & 1) * 64)));
          hi[s] = __builtin_bit_cast(u32x2, __builtin_amdgcn_ds_read_tr16_b64_v4i16((LAS s16x4*)(lds + ya + (s >> 1) * 512 + (s & 1) * 64 + 256)));
        }
        CB();
        if (__builtin_amdgcn_readfirstlane(f.x & f.y & f.z & f.w)) break;
      }
#pragma unroll
      for (int s = 0; s < 4; ++s)
        st128<X2S>(u32x4{lo[s].x | 1u, lo[s].y, hi[s].x | 1u, hi[s].y}, rws, yoff, s * 1024);
      SB();
      LDS32(L_SYN + 64 + w * 4) = 1u;
      ldw();
      STAMP(7); STAMPY(7);
      asm volatile("" : "={v[80:83]}"(qa[0][0]), "={v[84:87]}"(qa[0][1]), "={v[88:91]}"(qa[1][0]), "={v[92:95]}"(qa[1][1]), "={v[78:79]}"(fl));
    } else {
      u64 sa[4];
      const u32 u = (fj - NFX) * 4 + (w & 3);
#pragma unroll
      for (int i = 0, b0 = 56 * FSH; i < NS; b0 += 8 * NN[i] + 64 * RN[i], ++i) {
        sa[i] = stripa(umin(b0 + (rowb ? 8 * NN[i] + (u - 8) * RN[i] : u * NN[i]) + umin(lane >> 3, (rowb ? RN[i] : NN[i]) - 1), 2047));
        if (RN[i] == 0 && rowb) { sa[i] = kwr; KEEP(sa[i]); }
      }
      const rsrc_t rq = mk(q + (size_t)rbk * 16 * D, rowb ? 16 * D * 2 : 0);
      auto ldq = [&]() __attribute__((always_inline)) {
        asm volatile("buffer_load_dwordx4 v[80:83], %4, %5, 0 offen\n\tbuffer_load_dwordx4 v[84:87], %4, %5, 0 offen offset:128\n\t"
                     "buffer_load_dwordx4 v[88:91], %4, %5, %6 offen\n\tbuffer_load_dwordx4 v[92:95], %4, %5, %6 offen offset:128"
                     : "=&{v[80:83]}"(qa[0][0]), "=&{v[84:87]}"(qa[0][1]), "=&{v[88:91]}"(qa[1][0]), "=&{v[92:95]}"(qa[1][1])
                     : "v"(vq), "s"(rq), "s"(8192) : "memory");
        SB();
      };
      u32x4 qb[2][2];
      u32x2 fe;
      auto ldqb = [&]() __attribute__((always_inline)) {
        asm volatile("buffer_load_dwordx4 v[96:99], %4, %5, 0 offen\n\tbuffer_load_dwordx4 v[100:103], %4, %5, 0 offen offset:128\n\t"
                     "buffer_load_dwordx4 v[104:107], %4, %5, %6 offen\n\tbuffer_load_dwordx4 v[108:111], %4, %5, %6 offen offset:128"
                     : "=&{v[96:99]}"(qb[0][0]), "=&{v[100:103]}"(qb[0][1]), "=&{v[104:107]}"(qb[1][0]), "=&{v[108:111]}"(qb[1][1])
                     : "v"(vq - 1024), "s"(rq), "s"(8192) : "memory");
        SB();
      };
      if (TQE) while ((u32)__builtin_amdgcn_s_memrealtime() - tb < TQE) __builtin_amdgcn_s_sleep(1);
      SB();
      if (QH0 < 0) ldqb();
      if (QH < 0) ldq();
      const u32x2 wr = ld64<0>(mk(w_r, rowb ? D * 2 : 0), (tid - 256) * 8);
      SB();
      recv(wpe, [&]() __attribute__((always_inline)) {
        LDS64(L_XS + (tid - 256) * 8) = wr;
        *(LAS float*)(lds + L_SYN + 192 + (w & 3) * 4) = wave_sum(dot2(wr.x, 0x3f803f80u, dot2(wr.y, 0x3f803f80u, 0.f)));
      });
      BAR();
      if (tid == 256) __hip_atomic_store((GAS u64*)(ws + WS_FLAG) + b, tag, __ATOMIC_RELAXED, __HIP_MEMORY_SCOPE_AGENT);
      if (HSL) __builtin_amdgcn_s_sleep(HSL);
      SB();
      auto stats = [&](auto n) __attribute__((always_inline)) {
        if (HP2) __builtin_amdgcn_s_setprio(HP2);
        QWAIT(n());
        if (!SHF) asm volatile("" : "+{v[96:99]}"(qb[0][0]), "+{v[100:103]}"(qb[0][1]), "+{v[104:107]}"(qb[1][0]), "+{v[108:111]}"(qb[1][1]));
        STAMPH(15); STAMPW(10, 5); STAMPW(11, 6); STAMPW(12, 7);
        if (rowb) {
          if (EFL) {
            asm volatile("" : "+{v[76:77]}"(fe));
            early = __all(((fe.x ^ (u32)tag) | (fe.y ^ (u32)(tag >> 32))) == 0u);
            if (early) LDS32(L_SYN + w * 4) = 1u;
          }
          if (!SHF) {
#pragma unroll
            for (int i = 0; i < 4; ++i) LDS128(qla + i * 4096) = qb[i >> 1][i & 1];
            qsum(qb, true);
          }
          ln1a();
          ldw();
        }
        STAMPH(29); STAMPW(14, 5); STAMPW(31, 7);
        if (HP2) __builtin_amdgcn_s_setprio(0);
        SB();
      };
      if (QH0 == 0) ldqb();
      if (QH == 0) ldq();
      while (NOW() < T2 - T2E) __builtin_amdgcn_s_sleep(1);
      SB();
      if (EFL) {
        asm volatile("buffer_load_dwordx2 v[76:77], %1, %2, 0 offen sc1" : "=&{v[76:77]}"(fe) : "v"(WS_F2 + rbk * 128 + ru * 8), "s"(rws) : "memory");
        SB();
      }
      if (QH0 == 1) ldqb();
      if (QH == 1) ldq();
      if (HS2 == 99) { LDS32(L_SYN + 208 + (w & 3) * 4) = 1u; waitsyn(208); }
      else if (HS2) __builtin_amdgcn_s_sleep(HS2);
      if (TS0 && NOW() > TS0) { sa[0] = kwr; KEEP(sa[0]); }
      SB();
      wpf0 = strip(sa[0]);
      STAMPH(26);
      if (QH0 == 2) ldqb();
      if (QH == 2) ldq();
      if (TS1 && NOW() > TS1) { sa[1] = kwr; KEEP(sa[1]); }
      SB();
      wpf1 = strip(sa[1]);
      STAMPH(27);
      if (G4E) wpf4 = strip(sa[2]);
      if (TS2 && NOW() > TS2) { sa[2] = kwr; KEEP(sa[2]); }
      SB();
      if (QH0 == 3) ldqb();
      if (QH == 3) ldq();
      constexpr int NQ = QH < 2 ? 2 : 3 - QH;
      if (SHF) {
        asm volatile("s_waitcnt vmcnt(%4)" : "+{v[96:99]}"(qb[0][0]), "+{v[100:103]}"(qb[0][1]), "+{v[104:107]}"(qb[1][0]), "+{v[108:111]}"(qb[1][1]) : "n"(6 + EFL + G4E) : "memory");
#pragma unroll
        for (int i = 0; i < 4; ++i) LDS128(qla + i * 4096) = qb[i >> 1][i & 1];
        qsum(qb, true);
#pragma unroll
        for (int r = 0; r < 2; ++r) { KEEP(s1q[r]); KEEP(s2q[r]); }
        SB();
        STAMPH(35);
      }
      if (SPOS == 0) stats([] { return NQ; });
      flags0();
      if (VS2) {
        u32 vc;
        asm volatile("s_getreg_b32 %0, hwreg(HW_REG_IB_STS, 0, 4)" : "=s"(vc));
        if (vc > VS2) { sa[2] = kwr; KEEP(sa[2]); }
        SB();
      }
      if (NS > 2 && !G4E) wpf4 = strip(sa[2]);
      STAMPH(28);
      if (NS > 3) wpf5 = strip(sa[3]);
      STAMPH(13);
      if (SPOS == 1) stats([] { return NQ + NS - 1; });
      if (rowb) {
        if (TLN) {
          for (;;) {
            CB();
            const u32x4 f = *(volatile LAS u32x4*)(lds + L_SYN + 128);
            late = NOW() >= TLN;
            if (late || __builtin_amdgcn_readfirstlane(f.x & f.y & f.z & f.w)) break;
            __builtin_amdgcn_s_sleep(1);
          }
          SB();
        } else waitsyn(128);
        STAMPH(30);
        __builtin_amdgcn_s_setprio(HPR);
        if (!late) ln1b();
      }
      STAMPH(16);
      if (!early) flags1([] { return FLW; });
      while (NOW() < (YSP == 4 ? YG0 - 8 : TH)) __builtin_amdgcn_s_sleep(1);
      SB();
      if (YWS && rowb) waitsyn(64);
    }
    if (rowb) {
      f32x2 hf[2][8];
      u32x4 hp[2][2], lp[2][2], op[2][2];
      u32 bkey, tlast, wpf6 = 0;
      float c2[2], m2[2];
      {
        f32x2 qf[2][8], lf[2][8];
        float wsum;
        u32x4 yin[2][2];
        bool pre;
        for (;;) {
          const u32x4 f = *(volatile LAS u32x4*)(lds + L_SYN + 16);
          const u32 g = *(volatile LAS u32*)(lds + fla + 4);
          pre = YSP == 4 || NOW() >= TY || !__builtin_amdgcn_readfirstlane(g);
          if (__builtin_amdgcn_readfirstlane(f.x & f.y & f.z & f.w)) break;
        }
#define YLD(k, reg, so) { while (NOW() < (k == 0 ? YG0 : k == 1 ? YG1 : k == 2 ? YG2 : YG3)) {} CB(); \
        asm volatile("buffer_load_dwordx4 " reg ", %1, %2, %3 offen sc1" : "+{" reg "}"(yin[k >> 1][k & 1]) : "v"(off), "s"(rws), "s"(so) : "memory"); SB(); }
#define YLDS(a, b) if (a <= 0 && 0 < b) YLD(0, "v[240:243]", 0) if (a <= 1 && 1 < b) YLD(1, "v[244:247]", 16 * XS) \
        if (a <= 2 && 2 < b) YLD(2, "v[248:251]", XS) if (a <= 3 && 3 < b) YLD(3, "v[252:255]", 17 * XS)
        asm volatile("" : "={v[240:243]}"(yin[0][0]), "={v[244:247]}"(yin[0][1]), "={v[248:251]}"(yin[1][0]), "={v[252:255]}"(yin[1][1]));
        STAMPY(10);
        if (pre) { YLDS(0, YSP) }
        auto pf3 = [&]() __attribute__((always_inline)) {
          SB();
          asm volatile("buffer_load_dword v239, %1, %2, 0 offen" : "={v239}"(wpf6) : "v"(dof), "s"(mk(wd, D * F * 2)) : "memory");
          SB();
        };
        if (TLN && late) { waitsyn(128); ln1b(); }
        STAMPY(13);
        {
          f32x4 cm;
          for (;;) {
            CB();
            const u32x2 f = *(volatile LAS u32x2*)(lds + fla);
            CB();
            cm = *(LAS f32x4*)(lds + cma);
            if (w < 4) {
#pragma unroll
              for (int i = 0; i < 4; ++i) qa[i >> 1][i & 1] = LDS128(qla + i * 4096);
            }
            CB();
            const u32 fx = f.x;
            wsum = __builtin_bit_cast(float, fx);
            if (__builtin_amdgcn_readfirstlane(f.y)) break;
          }
#pragma unroll
          for (int i = 0; i < 8; ++i)
#pragma unroll
            for (int r = 0; r < 2; ++r) {
              const int j = (i & 1) + (i & 2) * 2 + (i & 4) / 2;
              qf[r][j] = unpk(qa[r][j >> 2][j & 3]);
              lf[r][j] = __builtin_elementwise_fma(qf[r][j], f32x2{cm[2 * r], cm[2 * r]}, f32x2{cm[2 * r + 1], cm[2 * r + 1]});
              KEEP(qf[r][j]); KEEP(lf[r][j]);
            }
          KEEP(wsum);
        }
        SB();
        STAMP(3); STAMPY(3);
        if (!pre) { YLDS(0, YSP) }
        YLDS(YSP, 4)
        if (P3K < 0) pf3();
        STAMPY(2);
        float s1[2] = {0.f, 0.f}, s2[2] = {0.f, 0.f}, sw[2] = {0.f, 0.f};
#pragma unroll
        for (int r = 0; r < 2; ++r) { KEEP(s1[r]); KEEP(s2[r]); KEEP(sw[r]); }
        SB();
#pragma unroll
        for (int k = 0; k < 4; ++k) {
          const int e = k >> 1, h = k & 1;
          {
            u32 t, n;
            u64 any;
#define YRE(a, c, d, so) "v_bitop3_b32 %[t], v" #a ", 1, v" #c " bitop3:0x80\n\tv_cmp_eq_u32 vcc, 0, %[t]\n\ts_or_b64 %[any], %[any], vcc\n\t" \
                         "s_mov_b64 exec, vcc\n\tbuffer_load_dwordx4 v[" #a ":" #d "], %[off], %[rs], " so " offen sc1\n\ts_mov_b64 exec, -1\n\t"
#define YCK(cnt, a, c) "s_mov_b32 %[n], 0\n\ts_waitcnt vmcnt(%[c" #cnt "])\n\tv_bitop3_b32 %[t], v" #a ", 1, v" #c " bitop3:0x80\n\t" \
            "v_cmp_eq_u32 vcc, 0, %[t]\n\ts_cbranch_vccz 2f\n" \
            "1:\n\ts_waitcnt vmcnt(0)\n\ts_mov_b64 %[any], 0\n\t" \
            YRE(240, 242, 243, "0") YRE(244, 246, 247, "%[o1]") YRE(248, 250, 251, "%[o2]") YRE(252, 254, 255, "%[o3]") \
            "s_cmp_eq_u64 %[any], 0\n\ts_cbranch_scc1 2f\n\ts_add_u32 %[n], %[n], 1\n\ts_cmp_lt_u32 %[n], %[cap]\n\ts_cbranch_scc1 1b\n\t" \
            "s_waitcnt vmcnt(0)\n2:"
#define YOPS : [t] "=&v"(t), [n] "=&s"(n), [any] "=&s"(any), "+{v[240:243]}"(yin[0][0]), "+{v[244:247]}"(yin[0][1]), "+{v[248:251]}"(yin[1][0]), \
               "+{v[252:255]}"(yin[1][1]) \
             : [off] "v"(off), [rs] "s"(rws), [o1] "s"(16 * XS), [o2] "s"(XS), [o3] "s"(17 * XS), [cap] "s"(SPIN_CAP), \
               [c3] "n"(3 + (P3K < 0)), [c2] "n"(2 + (P3K < 1)), [c1] "n"(1 + (P3K < 2)), [c0] "n"(0 + (P3K < 3)) : "memory", "scc", "vcc"
            if (k == 0) { if (early) flags1([] { return 4 + (P3K < 0); }); asm volatile(YCK(3, 240, 242) YOPS); }
            else if (k == 1) asm volatile(YCK(2, 244, 246) YOPS);
            else if (k == 2) asm volatile(YCK(1, 248, 250) YOPS);
            else { asm volatile(YCK(0, 252, 254) YOPS); if (P3K > 2) { KEEP(wpf0); KEEP(wpf1); KEEP(wpf4); KEEP(wpf5); } }
            if (k == P3K) pf3();
          }
          STAMPY(26 + k);
          SB();
#pragma unroll
          for (int d = 0; d < 2; ++d)
#pragma unroll
            for (int r = 0; r < 2; ++r) {
              const int i = h * 4 + e * 2 + d;
              hf[r][i] = __builtin_elementwise_fma(lf[r][i], unpk(yin[e][h][2 * r + d]), qf[r][i]);
              const u32 x = pk(hf[r][i]);
              hp[r][h][e * 2 + d] = x;
              s1[r] = dot2(x, 0x3f803f80u, s1[r]);
              s2[r] = dot2(x, x, s2[r]);
              sw[r] = dot2(x, wa[h][e * 2 + d], sw[r]);
            }
#pragma unroll
          for (int r = 0; r < 2; ++r) { KEEP(s1[r]); KEEP(s2[r]); KEEP(sw[r]); }
          SB();
        }
        STAMPY(4);
        rsum0(s1, s2, sw, 2048);
        float mean, msq, swt;
        rsum1(mean, msq, swt, 2048);
        STAMP(8); STAMPY(8);
        mean *= 1.f / D;
        const float mb = rbf(mean), rs = rstd(fmaxf(fmaf(-mean, mean, msq * (1.f / D)), 0.f));
        u32 rbits = pack2(0.f, fmaxf((swt - mb * wsum) * rs, -__builtin_inff()));
        asm volatile("v_add_f32 %0, 0, %0" : "+v"(rbits));
        u32 key = ((rbits ^ ((u32)((int)rbits >> 31) | 0x80000000u)) & 0xffff0000u) | ridx;
        key = umax(key, dppu<XOR1>(key)); key = umax(key, dppu<XOR2>(key)); key = umax(key, dppu<XOR7>(key));
        bkey = umax(__builtin_amdgcn_readlane(key, 0), __builtin_amdgcn_readlane(key, 32));
        both(rs, c2); both(-mb * rs, m2);
      }
      STAMP(9); STAMPY(9);
      const int brow = (2047 - (int)(bkey & 2047u)) & 15;
      if (w == KW) st128<SC1>(u32x4{bkey, bkey, bkey, bkey}, rws, kso);
      SB();
      tlast = NOW();
      {
        const u32 co = ru == ((brow >> 3) * 4 + (brow & 3)) ? WS_CAND + rbk * 4096 + vw : 0x80000000u;
#pragma unroll
        for (int r = 0; r < 2; ++r)
          if (((brow >> 2) & 1) == r) {
#pragma unroll
            for (int i = 0; i < 8; ++i) lp[r][i >> 2][i & 3] = pk(__builtin_elementwise_fma(hf[r][i], f32x2{c2[r], c2[r]}, f32x2{m2[r], m2[r]}));
#pragma unroll
            for (int h = 0; h < 2; ++h) st128<SC1>(guard(lp[r][h]), rws, co + h * 128);
#pragma unroll
            for (int h = 0; h < 2; ++h) st128<SC1>(guard(hp[r][h]), rws, co + 2048 + h * 128);
            CB();
          }
        SB();
        if (KTD && w == 2) { const u32 x = tlast + tb; st128<SC1>(u32x4{x, x, x, x}, rws, lane < TREP ? kso + 2048 : 0x80000000u); SB(); }
        if (WP3 == 2 && !TP3 && P3K == 4) { if (WE == 0) wpf1 = ld32<0>(mk(wd, D * F * 2), dof - 256); wpf0 = ld32<0>(mk(wd, D * F * 2), dof); SB(); }
        if (b == 128 && tid == 192) __hip_atomic_store((GAS u64*)(ws + WS_E), tag, __ATOMIC_RELAXED, __HIP_MEMORY_SCOPE_AGENT);
#pragma unroll
        for (int r = 0; r < 2; ++r) {
#pragma unroll
          for (int i = 0; i < 8; ++i)
            op[r][i >> 2][i & 3] = pk(hf[r][i] + __builtin_elementwise_fma(hf[r][i], f32x2{c2[r], c2[r]}, f32x2{m2[r], m2[r]}));
          KEEP(op[r][0]); KEEP(op[r][1]);
        }
        SB();
      }
      if (WP3 == 2 && TP3) { while (NOW() < TP3) __builtin_amdgcn_s_sleep(1); SB(); wpf0 = ld32<0>(mk(wd, D * F * 2), dof); SB(); }
      if (w == PW) top1(bkey, rbk, umax(TK + TKO, tlast + TK2));
      u32 gk;
      while (!(gk = __builtin_amdgcn_readfirstlane(*(volatile LAS u32*)(lds + L_KEY + 32)))) __builtin_amdgcn_s_sleep(1);
      KEEP(wpf0); if (WE == 0) KEEP(wpf1);
      if (P3K < 4) { asm volatile("s_waitcnt vmcnt(0)" : "+{v239}"(wpf6) :: "memory"); KEEP(wpf1); KEEP(wpf4); KEEP(wpf5); }
      if (WP3 >= 3) { KEEP(wpf1); KEEP(wpf4); KEEP(wpf5); KEEP(wpf6); }
      STAMP(25); STAMPY(25);
#pragma unroll
      for (int r = 0; r < 2; ++r) {
        const u32 oo = ((ridx + (lane >> 5) * 4 - 4 * r) ^ gk) & 2047u ? (u32)(rbk * 16 + 4 * r) * 2048u + vq : 0x80000000u;
#pragma unroll
        for (int h = 0; h < 2; ++h) st128<OAUX>(op[r][h], ro, (XPO == 1 ? 0x80000000u : oo) + h * 128);
      }
    } else {
      KEEP(wpf0); KEEP(wpf1); KEEP(wpf4); KEEP(wpf5);
      if (w == PW) {
        u32 tka = 0;
        if (CKK) tka = clk1(CKK, CKKC);
        while (NOW() < TF2) __builtin_amdgcn_s_sleep(1);
        for (int spins = 0; spins < SPIN_CAP; ++spins) {
          CB();
          u32x4 f[2];
#pragma unroll
          for (int i = 0; i < 2; ++i) f[i] = ld128<SC1>(rws, WS_FLAG + lane * 16, i * 1024) ^ u32x4{(u32)tag, (u32)(tag >> 32), (u32)tag, (u32)(tag >> 32)};
          if (__all(((f[0].x | f[0].y | f[0].z) | (f[0].w | f[1].x | f[1].y) | (f[1].z | f[1].w)) == 0u)) break;
        }
        top1(SENT, -1, CKC ? TKA : TK + (CKK ? tka : 0));
      }
      if (w == FW && XPO != 2) {
        __builtin_amdgcn_s_setprio(0);
        constexpr u32 PST = 8 * XS;
        const u32 col = rbk * 8 + (lane & 1) * 4 + ((lane >> 4) & 1) * 2 + (lane >> 5);
        u32 off = WS_X + (u32)(((lane >> 1) & 7) * 32 + (lane >> 4) * 2 + (rbk >> 6)) * XS + ((lane >> 1) & 7) * 2048 + (rbk & 63) * 32 + (lane & 1) * 16;
        u32 off3 = lane < 32 ? off + 3 * PST : 0x80000000u, oo = (lane & 14) ? 0x80000000u : col * 2, ho = (col >> 1) * 4;
        KEEP(off); KEEP(off3); KEEP(oo); KEEP(ho);
        SB();
        u32 gkey;
        while (!(gkey = __builtin_amdgcn_readfirstlane(*(volatile LAS u32*)(lds + L_KEY + 32)))) __builtin_amdgcn_s_sleep(1);
        CB();
        STAMPY(39);
        const u32 grow = 2047u - (gkey & 2047u);
        const u32 t0_ = (u32)__builtin_amdgcn_s_memtime();
        float sg, lv, hv;
        {
          u32 x = SENT;
#pragma unroll 1
          for (int spins = 0; spins < SPIN_CAP; ++spins) {
            CB();
            x = ld32<SC1>(rws, ho, WS_CAND + (grow >> 4) * 4096 + 2048);
            if (__all(x != SENT)) break;
          }
          const u32 ord = gkey >> 16, l2 = LDS32(L_XS + ho);
          sg = rbf(__builtin_amdgcn_rcpf(1.f + __expf(-bflo((ord & 0x8000u) ? (ord & 0x7fffu) : (~ord & 0xffffu)))));
          hv = lane < 32 ? bflo(x) : bfhi(x); lv = lane < 32 ? bflo(l2) : bfhi(l2);
        }
        KEEP(sg); KEEP(lv); KEEP(hv);
        SB();
        while ((u32)__builtin_amdgcn_s_memtime() - t0_ < DLY4 * 64) __builtin_amdgcn_s_sleep(1);
        STAMPY(40);
        f32x2 s01, s23;
        {
          u32 t, u, n;
#define PLD(a, o, so) "buffer_load_dwordx4 v[" #a ":" #a "+3], " o ", %[rs], " so " offen sc1\n\t"
#define PALL(a) PLD(a, "%[off]", "0") PLD(a+4, "%[off]", "%[o1]") PLD(a+8, "%[off]", "%[o2]") PLD(a+12, "%[of3]", "0")
#define PRE(a, o, so) "v_bitop3_b32 %[t], v[" #a "], 1, v[" #a "+2] bitop3:0xc8\n\tv_cmp_ne_u32 vcc, 0, %[t]\n\ts_or_b64 exec, vcc, 1\n\t" PLD(a, o, so) "s_mov_b64 exec, -1\n\t"
#define PCK(a, cnt, lab) "s_waitcnt vmcnt(" cnt ")\n\tv_or3_b32 %[t], v[" #a "], v[" #a "+2], v[" #a "+4]\n\tv_or3_b32 %[u], v[" #a "+6], v[" #a "+8], v[" #a "+10]\n\t" \
          "v_or3_b32 %[t], v[" #a "+12], v[" #a "+14], %[t]\n\tv_bitop3_b32 %[t], %[t], 1, %[u] bitop3:0xc8\n\tv_cmp_ne_u32 vcc, 0, %[t]\n\ts_cbranch_vccz " lab "f\n\t" \
          PRE(a, "%[off]", "0") PRE(a+4, "%[off]", "%[o1]") PRE(a+8, "%[off]", "%[o2]") PRE(a+12, "%[of3]", "0")
#define PAD(d, x, y) "v_pk_add_f32 " d ", v[" #x ":" #x "+1], v[" #y ":" #y "+1]\n\t"
#define PSUM(a, lab) lab ":\n\t" PAD("v[" #a ":" #a "+1]", a, a+4) PAD("v[" #a "+8:" #a "+9]", a+8, a+12) PAD("v[" #a "+2:" #a "+3]", a+2, a+6) \
          PAD("v[" #a "+10:" #a "+11]", a+10, a+14) PAD("%[s01]", a, a+8) PAD("%[s23]", a+2, a+10)
#define PLOOP "s_add_u32 %[n], %[n], 1\n\ts_cmp_lt_u32 %[n], %[cap]\n\ts_cbranch_scc1 1b\n\ts_waitcnt vmcnt(0)\n"
          asm volatile("s_mov_b32 %[n], 0\n\t"
                       PALL(100) "s_sleep %[d]\n\t" PALL(116) "s_sleep %[d]\n\t" PALL(132) "1:\n\t" PCK(100, "8", "2") PCK(116, "8", "3") PCK(132, "8", "4") PLOOP
                       PSUM(132, "4") "s_branch 9f\n" PSUM(116, "3") "s_branch 9f\n" PSUM(100, "2") "9:"
                       : [t] "=&v"(t), [u] "=&v"(u), [n] "=&s"(n), [s01] "=&v"(s01), [s23] "=&v"(s23)
                       : [off] "v"(off), [of3] "v"(off3), [rs] "s"(rws), [o1] "s"(PST), [o2] "s"(2 * PST), [cap] "s"(SPIN_CAP), [d] "n"(PDL)
                       : "memory", "scc", "vcc", PCLOB);
        }
        STAMPY(41);
        {
          const float a0 = s01.x, a1 = s01.y, a2 = s23.x, a3 = s23.y;
          float y = h16(h32(a0, a1), h32(a2, a3));
          y += dppf<XOR8>(y); y += dppf<0x124>(y); y += dppf<XOR2>(y);
          const u32 r = pack2(0.f, hv + rbf(lv + rbf(rbf(y) * sg)));
          asm volatile("buffer_store_short_d16_hi %0, %1, %2, %3 offen" :: "v"(r), "v"(oo), "s"(ro), "s"(grow * 2048) : "memory");
          STAMPY(43);
        }
        asm volatile("" ::: PCLOB);
      }
    }
  }
  STAMP_DUMP();
  asm volatile(".Lk_all_end:");
}

extern "C" void kernel_launch(void* const* d_in, const int* in_sizes, int n_in, void* d_out, int out_size,
                              void* d_ws, size_t ws_size, hipStream_t stream) {
  k_all<<<NB, NT, 0, stream>>>(d_in[1], d_in[2], d_in[3], d_in[4], d_ws, d_in[5], d_in[0], d_in[7], d_in[8], d_in[9], d_in[10], d_out);
}
```
